```python
import torch

def _softmax(x, axis=-1):
    return torch.softmax(x, dim=axis)

def _gelu(x):
    return torch.nn.functional.gelu(x, approximate='tanh')

def _silu(x):
    return torch.nn.functional.silu(x)

def _sigmoid(x):
    return torch.sigmoid(x)

def _layernorm(x, axis=-1, eps=1e-05):
    m = x.mean(dim=axis, keepdim=True)
    v = x.var(dim=axis, keepdim=True, unbiased=False)
    return (x - m) / torch.sqrt(v + eps)

def _scatter_add(tgt, idx, src, axis=0):
    return tgt.clone().index_add_(axis, idx, src)

def _segment_sum(src, seg, n):
    out = torch.zeros((n,) + src.shape[1:], dtype=src.dtype)
    return out.index_add_(0, seg, src)

def _pad(x, pw):
    flat = tuple((v for pair in reversed(pw) for v in pair))
    return torch.nn.functional.pad(x, flat)
kernel_name = 'adaLN_cond__ssm_conv_s4d__glu_gated_geglu__prompt'

def setup_inputs():

    _w = lambda a: (lambda t: t.to(torch.bfloat16) if t.is_floating_point() else t)(torch.as_tensor(a))

    def _A_setup():
        Bsz, L, D, N = (1, 2048, 512, 128)
        return {'u': _w((lambda *s: torch.randn(*s, dtype=torch.bfloat16) * 0.05)(Bsz, L, D)), 'log_A_real': _w((lambda *s: torch.randn(*s, dtype=torch.bfloat16) * 3.2)(D, N) * 0.5), 'A_imag': _w((lambda *s: torch.randn(*s, dtype=torch.bfloat16) * 0.1)(D, N) * 3.14), 'C_real': _w((lambda *s: torch.randn(*s, dtype=torch.bfloat16) * 0.2)(D, N) / N ** 0.5), 'C_imag': _w((lambda *s: torch.randn(*s, dtype=torch.bfloat16) * 0.2)(D, N) / N ** 0.5), 'log_dt': _w((lambda *s: torch.randn(*s, dtype=torch.bfloat16) * 0.1)(D) * 0.5 - 4.0), 'D_skip': _w((lambda *s: torch.randn(*s, dtype=torch.bfloat16) * 0.1)(D))}
    _A_in = _A_setup()

    def _B_setup():
        B, S, Dm, Df = (1, 2048, 512, 1792)
        return {'x': _w((lambda *s: torch.randn(*s, dtype=torch.bfloat16) * 0.05)(B, S, Dm)), 'w_gate': _w((lambda *s: torch.randn(*s, dtype=torch.bfloat16) * 1.6)(Dm, Df) * Dm ** (-0.5)), 'w_up': _w((lambda *s: torch.randn(*s, dtype=torch.bfloat16) * 1.6)(Dm, Df) * Dm ** (-0.5)), 'w_down': _w((lambda *s: torch.randn(*s, dtype=torch.bfloat16) * 1.6)(Df, Dm) * Df ** (-0.5))}
    _B_in = _B_setup()
    return {'A_u': _A_in['u'], 'A_log_A_real': _A_in['log_A_real'], 'A_A_imag': _A_in['A_imag'], 'A_C_real': _A_in['C_real'], 'A_C_imag': _A_in['C_imag'], 'A_log_dt': _A_in['log_dt'], 'A_D_skip': _A_in['D_skip'], 'B_x': _B_in['x'], 'B_w_gate': _B_in['w_gate'], 'B_w_up': _B_in['w_up'], 'B_w_down': _B_in['w_down'], 'c': _w((lambda *s: torch.randn(*s, dtype=torch.bfloat16) * 0.2)(1, 512) * 8.0), 'w_mod': _w((lambda *s: torch.randn(*s, dtype=torch.bfloat16) * 0.2)(512, 6 * 512) * 512 ** (-0.5) * 8.0)}

def reference(A_u, A_log_A_real, A_A_imag, A_C_real, A_C_imag, A_log_dt, A_D_skip, B_x, B_w_gate, B_w_up, B_w_down, c, w_mod):

    def _A_ref(u, log_A_real, A_imag, C_real, C_imag, log_dt, D_skip):
        L = u.shape[1]
        dt = torch.exp(log_dt)
        A = -torch.exp(log_A_real) + 1j * A_imag
        C = (C_real + 1j * C_imag) * dt[:, None]
        pos = (lambda x, dt: x.to(dt))(torch.arange(L), log_dt.dtype)
        P = torch.exp((A * dt[:, None])[:, :, None] * pos[None, None, :])
        K = 2.0 * torch.einsum('dn,dnl->dl', C, P).real
        L2 = 2 * L
        u_t = (lambda x, *d: x.permute(*d))(u, 0, 2, 1)
        Kf = (lambda x, *a, **k: torch.fft.rfft(x.float(), *a, **k))(K, L2)
        Uf = (lambda x, *a, **k: torch.fft.rfft(x.float(), *a, **k))(u_t, L2)
        y = (lambda x, *a, **k: torch.fft.irfft(x, *a, **k).to(torch.bfloat16))(Uf * Kf[None, :, :], L2)[:, :, :L]
        y = (lambda x, *d: x.permute(*d))(y, 0, 2, 1)
        return y + u * D_skip[None, None, :]

    def _B_ref(x, w_gate, w_up, w_down):
        g = torch.einsum('bsd,df->bsf', x, w_gate)
        u = torch.einsum('bsd,df->bsf', x, w_up)
        h = _gelu(g) * u
        return torch.einsum('bsf,fd->bsd', h, w_down)
    x = A_u
    mod = (_silu(c) @ w_mod).reshape(c.shape[0], 6, 1, -1)
    sa, ca, ga, sb, cb, gb = (mod[:, 0].clone(), mod[:, 1].clone(), mod[:, 2].clone(), mod[:, 3].clone(), mod[:, 4].clone(), mod[:, 5].clone())
    h = _layernorm(x, axis=-1) * (1 + ca) + sa
    x = x + ga * _A_ref(h, A_log_A_real, A_A_imag, A_C_real, A_C_imag, A_log_dt, A_D_skip)
    h = _layernorm(x, axis=-1) * (1 + cb) + sb
    x = x + gb * _B_ref(h, B_w_gate, B_w_up, B_w_down)
    return x
```

```cpp
#include <hip/hip_runtime.h>
#include <hip/hip_bf16.h>
#include <stdint.h>

typedef unsigned short u16;
typedef unsigned int u32;
typedef u32 u32x2 __attribute__((ext_vector_type(2)));
typedef u32 u32x4 __attribute__((ext_vector_type(4)));
typedef float f32x2 __attribute__((ext_vector_type(2)));
typedef float f32x4 __attribute__((ext_vector_type(4)));
typedef float f32x16 __attribute__((ext_vector_type(16)));
typedef short s16x4 __attribute__((ext_vector_type(4)));
typedef __bf16 bf16x2 __attribute__((ext_vector_type(2)));
typedef __bf16 bf16x8 __attribute__((ext_vector_type(8)));
typedef _Float16 f16x2 __attribute__((ext_vector_type(2)));
typedef _Float16 f16x8 __attribute__((ext_vector_type(8)));
#define LAS __attribute__((address_space(3)))
#define DI __device__ __forceinline__

constexpr int L_ = 2048, D_ = 512, N_ = 128, F_ = 1792;
constexpr int KTAB = 768;
constexpr int NSLAB = F_ / 16;
constexpr int WKS = 33;

constexpr size_t OFF_MOD = 0;
constexpr size_t OFF_ST = 16384;
constexpr size_t OFF_KT = 32768;
constexpr size_t OFF_XT = OFF_KT + (size_t)D_ * KTAB * 2;
constexpr size_t OFF_X1 = OFF_XT + (size_t)D_ * L_ * 2;
constexpr size_t OFF_WP = OFF_X1 + (size_t)D_ * L_ * 2;
constexpr size_t OFF_WD = OFF_WP + (size_t)NSLAB * WKS * 1024;
constexpr size_t OFF_HH = OFF_WD + (size_t)16 * NSLAB * 1024;

DI float blo(u32 u) { return __uint_as_float(u << 16); }
DI float bhi(u32 u) { return __uint_as_float(u & 0xffff0000u); }
DI float bf(u16 h) { return __uint_as_float((u32)h << 16); }
DI u32 pkbf(float a, float b) {
  f32x2 v = {a, b};
  return __builtin_bit_cast(u32, __builtin_convertvector(v, bf16x2));
}
DI u32 pkh(float a, float b) {
  f32x2 v = {a, b};
  return __builtin_bit_cast(u32, __builtin_convertvector(v, f16x2));
}
DI float rbf(float a) { return blo(pkbf(a, a)); }
DI u32 perm_lo(u32 hi, u32 lo) { return __builtin_amdgcn_perm(hi, lo, 0x05040100u); }
DI u32 perm_hi(u32 hi, u32 lo) { return __builtin_amdgcn_perm(hi, lo, 0x07060302u); }
DI f32x4 mfma16(u32x4 a, u32x4 b, f32x4 c) {
  return __builtin_amdgcn_mfma_f32_16x16x32_bf16(__builtin_bit_cast(bf16x8, a), __builtin_bit_cast(bf16x8, b), c, 0, 0, 0);
}
DI f32x4 mfma16h(u32x4 a, u32x4 b, f32x4 c) {
  return __builtin_amdgcn_mfma_f32_16x16x32_f16(__builtin_bit_cast(f16x8, a), __builtin_bit_cast(f16x8, b), c, 0, 0, 0);
}
DI f32x16 mfma32(u32x4 a, u32x4 b, f32x16 c) {
  return __builtin_amdgcn_mfma_f32_32x32x16_bf16(__builtin_bit_cast(bf16x8, a), __builtin_bit_cast(bf16x8, b), c, 0, 0, 0);
}
DI u32x2 tr_read(const char* p) {
  return __builtin_bit_cast(u32x2, __builtin_amdgcn_ds_read_tr16_b64_v4i16((LAS s16x4*)p));
}
DI float dot2bf(u32 a, u32 b, float c) {
  return __builtin_amdgcn_fdot2_f32_bf16(__builtin_bit_cast(bf16x2, a), __builtin_bit_cast(bf16x2, b), c, false);
}
template <int CTRL> DI float dpp_add(float v) {
  return v + __builtin_bit_cast(float, __builtin_amdgcn_update_dpp(0, __builtin_bit_cast(int, v), CTRL, 0xf, 0xf, true));
}
DI float wave_sum(float v) {
  v = dpp_add<0x111>(v);
  v = dpp_add<0x112>(v);
  v = dpp_add<0x114>(v);
  v = dpp_add<0x118>(v);
  float a = __builtin_bit_cast(float, __builtin_amdgcn_readlane(__builtin_bit_cast(int, v), 15));
  float b = __builtin_bit_cast(float, __builtin_amdgcn_readlane(__builtin_bit_cast(int, v), 31));
  float c = __builtin_bit_cast(float, __builtin_amdgcn_readlane(__builtin_bit_cast(int, v), 47));
  float d = __builtin_bit_cast(float, __builtin_amdgcn_readlane(__builtin_bit_cast(int, v), 63));
  return (a + b) + (c + d);
}
template <int CTRL> DI float dpp_max(float v) {
  return fmaxf(v, __builtin_bit_cast(float, __builtin_amdgcn_update_dpp(0, __builtin_bit_cast(int, v), CTRL, 0xf, 0xf, true)));
}
DI float wave_max_nonneg(float v) {
  v = dpp_max<0x111>(v);
  v = dpp_max<0x112>(v);
  v = dpp_max<0x114>(v);
  v = dpp_max<0x118>(v);
  float a = __builtin_bit_cast(float, __builtin_amdgcn_readlane(__builtin_bit_cast(int, v), 15));
  float b = __builtin_bit_cast(float, __builtin_amdgcn_readlane(__builtin_bit_cast(int, v), 31));
  float c = __builtin_bit_cast(float, __builtin_amdgcn_readlane(__builtin_bit_cast(int, v), 47));
  float d = __builtin_bit_cast(float, __builtin_amdgcn_readlane(__builtin_bit_cast(int, v), 63));
  return fmaxf(fmaxf(a, b), fmaxf(c, d));
}
struct cf { float r, i; };
DI cf cmul(cf a, cf b) { return {a.r * b.r - a.i * b.i, a.r * b.i + a.i * b.r}; }
DI cf cexp_p(float zr2, float zi2, float p) {
  float e = __builtin_amdgcn_exp2f(zr2 * p);
  float a = __builtin_amdgcn_fractf(zi2 * p);
  return {e * __builtin_amdgcn_cosf(a), e * __builtin_amdgcn_sinf(a)};
}

constexpr int K1_KGEN = 256, K1_MOD = 96, K1_XT = 128;
constexpr int UROW = 272, UWAVE = 57 * UROW;

__global__ __launch_bounds__(256) void k1(const u16* __restrict__ x, const u16* __restrict__ logA,
                                          const u16* __restrict__ Aim, const u16* __restrict__ Cre,
                                          const u16* __restrict__ Cim, const u16* __restrict__ log_dt,
                                          const u16* __restrict__ cvec, const u16* __restrict__ wmod,
                                          char* __restrict__ ws) {
  __shared__ __attribute__((aligned(16))) char sm[4 * UWAVE];
  const int t = threadIdx.x, w = t >> 6, lane = t & 63;
  int b = blockIdx.x;
  if (b < K1_MOD) {
    const int cb = (b & 7) * 12 + (b >> 3);
    const int rg = t >> 2, cc = t & 3;
    u32x4 wv[8];
    float sc[8];
#pragma unroll
    for (int i = 0; i < 8; ++i)
      wv[i] = *(const u32x4*)(wmod + (size_t)(rg + 64 * i) * 3072 + cb * 32 + cc * 8);
#pragma unroll
    for (int i = 0; i < 8; ++i) {
      float c = bf(cvec[rg + 64 * i]);
      sc[i] = rbf(c / (1.f + expf(-c)));
    }
    float acc[8];
#pragma unroll
    for (int j = 0; j < 8; ++j) acc[j] = 0.f;
#pragma unroll
    for (int i = 0; i < 8; ++i)
#pragma unroll
      for (int j = 0; j < 4; ++j) {
        acc[2 * j] += sc[i] * blo(wv[i][j]);
        acc[2 * j + 1] += sc[i] * bhi(wv[i][j]);
      }
    float* red = (float*)sm;
    *(f32x4*)(red + rg * 36 + cc * 8) = f32x4{acc[0], acc[1], acc[2], acc[3]};
    *(f32x4*)(red + rg * 36 + cc * 8 + 4) = f32x4{acc[4], acc[5], acc[6], acc[7]};
    __syncthreads();
    {
      const int col = t & 31, part = t >> 5;
      float s = 0.f;
#pragma unroll
      for (int i = 0; i < 8; ++i) s += red[(part * 8 + i) * 36 + col];
      red[64 * 36 + part * 32 + col] = s;
    }
    __syncthreads();
    if (t < 32) {
      float s = 0.f;
#pragma unroll
      for (int i = 0; i < 8; ++i) s += red[64 * 36 + i * 32 + t];
      ((float*)(ws + OFF_MOD))[cb * 32 + t] = rbf(s);
    }
    return;
  }
  b -= K1_MOD;
  if (b < K1_KGEN) {
    const int d = 2 * b + (w >> 1), n = (w & 1) * 64 + lane;
    const float lar = bf(logA[d * N_ + n]), ai = bf(Aim[d * N_ + n]);
    const float cr = bf(Cre[d * N_ + n]), ci = bf(Cim[d * N_ + n]);
    const float dt = rbf(expf(bf(log_dt[d])));
    const float ea = rbf(expf(lar));
    const float zr2 = (-ea * dt) * 1.44269504088896f, zi2 = (ai * dt) * 0.159154943091895f;
    char* base = sm + w * UWAVE;
    char* wr = base + lane * 4;
#define PUT(row, v) (*(u32*)(wr + (row) * UROW) = pkh((v).r, (v).i))
    cf E[8];
    E[0] = {1.f, 0.f};
    E[1] = cexp_p(zr2, zi2, 1.f);
    E[2] = cmul(E[1], E[1]);
    E[3] = cmul(E[2], E[1]);
    E[4] = cmul(E[2], E[2]);
    E[5] = cmul(E[4], E[1]);
    E[6] = cmul(E[4], E[2]);
    E[7] = cmul(E[4], E[3]);
    const cf W8 = cexp_p(zr2, zi2, 8.f);
    const cf W128 = cexp_p(zr2, zi2, 128.f);
#pragma unroll
    for (int i = 0; i < 8; ++i) PUT(i, E[i]);
    cf Wc = W128;
    PUT(8, Wc);
#pragma unroll
    for (int i = 1; i < 8; ++i) { cf u = cmul(Wc, E[i]); PUT(8 + i, u); }
#pragma unroll
    for (int i = 2; i <= 16; ++i) {
      Wc = cmul(Wc, W128);
      if (i < 4) {
        PUT(16 + 4 * (i - 2), Wc);
#pragma unroll
        for (int q = 1; q < 4; ++q) { cf u = cmul(Wc, E[2 * q]); PUT(16 + 4 * (i - 2) + q, u); }
      } else if (i < 8) {
        PUT(24 + 2 * (i - 4), Wc);
        cf u = cmul(Wc, E[4]);
        PUT(24 + 2 * (i - 4) + 1, u);
      } else {
        PUT(32 + (i - 8), Wc);
      }
    }
    const float m = wave_max_nonneg(fmaxf(fabsf(cr), fabsf(ci)));
    const int ex = __builtin_amdgcn_frexp_expf(m);
    cf V = {__builtin_amdgcn_ldexpf(cr, -ex), __builtin_amdgcn_ldexpf(ci, -ex)};
#pragma unroll
    for (int j = 0; j < 16; ++j) {
      *(u32*)(wr + (41 + j) * UROW) = pkh(V.r, -V.i);
      if (j < 15) V = cmul(V, W8);
    }
#undef PUT
    const int r = lane & 15, g = lane >> 4;
    f32x4 acc[3];
#pragma unroll
    for (int mt = 0; mt < 3; ++mt) acc[mt] = f32x4{0.f, 0.f, 0.f, 0.f};
    const char* rd = base + r * UROW + g * 16;
#pragma unroll
    for (int ks = 0; ks < 4; ++ks) {
      const u32x4 vb = *(const u32x4*)(rd + 41 * UROW + ks * 64);
#pragma unroll
      for (int mt = 0; mt < 3; ++mt) {
        const u32x4 ua = *(const u32x4*)(rd + mt * 16 * UROW + ks * 64);
        acc[mt] = mfma16h(ua, vb, acc[mt]);
      }
    }
    const float fac = __builtin_amdgcn_ldexpf(2.f * dt, ex);
    float* tab = (float*)base;
#pragma unroll
    for (int q = 0; q < 4; ++q) {
      const int row = 4 * g + q;
      tab[128 * (row >> 3) + (row & 7) + 8 * r] = acc[0][q] * fac;
      const int i1 = row < 8 ? 256 + 64 * (row >> 2) + (row & 3) + 4 * r : 384 + 32 * ((row >> 1) & 3) + (row & 1) + 2 * r;
      tab[i1] = acc[1][q] * fac;
      tab[512 + 16 * row + r] = acc[2][q] * fac;
    }
    __syncthreads();
    const int ch = t >> 7, u = t & 127;
    if (u < 82) {
      const float* t0 = (const float*)(sm + (2 * ch) * UWAVE) + 8 * u;
      const float* t1 = (const float*)(sm + (2 * ch + 1) * UWAVE) + 8 * u;
      const f32x4 a0 = *(const f32x4*)t0, a1 = *(const f32x4*)(t0 + 4);
      const f32x4 b0 = *(const f32x4*)t1, b1 = *(const f32x4*)(t1 + 4);
      u32x4 o = {pkbf(a0[0] + b0[0], a0[1] + b0[1]), pkbf(a0[2] + b0[2], a0[3] + b0[3]),
                 pkbf(a1[0] + b1[0], a1[1] + b1[1]), pkbf(a1[2] + b1[2], a1[3] + b1[3])};
      *(u32x4*)(ws + OFF_KT + ((size_t)(2 * b + ch) * KTAB + 8 * u) * 2) = o;
    }
    return;
  }
  b -= K1_KGEN;
  const int q = 2 * b + (w & 1);
  if (w < 2) {
    const int rb = q >> 3, cb = q & 7, rg = lane >> 3, cg = lane & 7;
    u32x4 v[8];
#pragma unroll
    for (int i = 0; i < 8; ++i) v[i] = *(const u32x4*)(x + (size_t)(64 * rb + 8 * rg + i) * D_ + 64 * cb + 8 * cg);
    u16* xT = (u16*)(ws + OFF_XT);
#pragma unroll
    for (int j = 0; j < 8; ++j) {
      u32x4 o;
#pragma unroll
      for (int m = 0; m < 4; ++m)
        o[m] = (j & 1) ? perm_hi(v[2 * m + 1][j >> 1], v[2 * m][j >> 1]) : perm_lo(v[2 * m + 1][j >> 1], v[2 * m][j >> 1]);
      *(u32x4*)(xT + (size_t)(64 * cb + 8 * cg + j) * L_ + 64 * rb + 8 * rg) = o;
    }
  } else {
    u32x4 v[8];
#pragma unroll
    for (int i = 0; i < 8; ++i) v[i] = *(const u32x4*)(x + (size_t)(8 * q + i) * D_ + 8 * lane);
    u32 res = 0;
#pragma unroll
    for (int i = 0; i < 8; ++i) {
      float s = 0.f;
#pragma unroll
      for (int j = 0; j < 4; ++j) s += blo(v[i][j]) + bhi(v[i][j]);
      const float mean = wave_sum(s) * (1.f / D_);
      float ss = 0.f;
#pragma unroll
      for (int j = 0; j < 4; ++j) {
        const float a = blo(v[i][j]) - mean, c = bhi(v[i][j]) - mean;
        ss += a * a + c * c;
      }
      const float var = rbf(wave_sum(ss) * (1.f / D_));
      const float sd = sqrtf(rbf(var + 1e-5f));
      const u32 pk = pkbf(mean, sd);
      if (lane == i) res = pk;
    }
    if (lane < 8) ((u32*)(ws + OFF_ST))[8 * q + lane] = res;
  }
}

constexpr int K2_CONV = 256, K2_WP = 112, K2_WD = 56;
constexpr int HB = 2304 * 2;
constexpr int KC = 2080 * 2;
constexpr int S2_H = 0, S2_K = 2 * HB, S2_T = S2_K + 4 * KC, S2_R = S2_T + 2 * KTAB * 2, S2_END = S2_R + 4 * 8192;

__global__ __launch_bounds__(256) void k2(const u16* __restrict__ dskip, const u16* __restrict__ wg,
                                          const u16* __restrict__ wu, const u16* __restrict__ wd,
                                          char* __restrict__ ws) {
  __shared__ __attribute__((aligned(16))) char sm[S2_END];
  const int t = threadIdx.x, w = t >> 6, lane = t & 63;
  int b = blockIdx.x;
  const float* mod = (const float*)(ws + OFF_MOD);
  if (b < K2_CONV) {
    const int d0 = 2 * b;
    const u16* xT = (const u16*)(ws + OFF_XT);
    u32x4 kt = {0u, 0u, 0u, 0u};
    if (t < 192) kt = *(const u32x4*)(ws + OFF_KT + (size_t)d0 * KTAB * 2 + t * 16);
    u32x4 xv[2];
    xv[0] = *(const u32x4*)(xT + (size_t)d0 * L_ + 8 * t);
    xv[1] = *(const u32x4*)(xT + (size_t)(d0 + 1) * L_ + 8 * t);
    const u32x4 st0 = *(const u32x4*)(ws + OFF_ST + t * 32);
    const u32x4 st1 = *(const u32x4*)(ws + OFF_ST + t * 32 + 16);
    const f32x2 sa = *(const f32x2*)(mod + d0), ca = *(const f32x2*)(mod + 512 + d0), ga = *(const f32x2*)(mod + 1024 + d0);
    const u32 dsk = *(const u32*)(dskip + d0);
    if (t < 64) *(u32x4*)(sm + S2_H + (t >> 5) * HB + (t & 31) * 16) = u32x4{0u, 0u, 0u, 0u};
    if (t < 192) *(u32x4*)(sm + S2_T + t * 16) = kt;
    u32x4 hv[2];
    {
      float mu[8], rs[8];
#pragma unroll
      for (int i = 0; i < 4; ++i) {
        mu[i] = blo(st0[i]); rs[i] = __builtin_amdgcn_rcpf(bhi(st0[i]));
        mu[4 + i] = blo(st1[i]); rs[4 + i] = __builtin_amdgcn_rcpf(bhi(st1[i]));
      }
#pragma unroll
      for (int c = 0; c < 2; ++c) {
        const float c1 = rbf(1.f + ca[c]);
#pragma unroll
        for (int j = 0; j < 4; ++j) {
          u32 p = pkbf(blo(xv[c][j]) - mu[2 * j], bhi(xv[c][j]) - mu[2 * j + 1]);
          p = pkbf(blo(p) * rs[2 * j], bhi(p) * rs[2 * j + 1]);
          p = pkbf(blo(p) * c1, bhi(p) * c1);
          hv[c][j] = pkbf(blo(p) + sa[c], bhi(p) + sa[c]);
        }
        *(u32x4*)(sm + S2_H + c * HB + 512 + t * 16) = hv[c];
      }
    }
    __syncthreads();
    {
      const int ch = t >> 7, u = t & 127;
      const u16* tab = (const u16*)(sm + S2_T) + ch * KTAB;
      u32 kv[17];
#pragma unroll
      for (int e = 0; e < 17; ++e) {
        const int tau = 2047 - 16 * u - e;
        const int idx = tau < 128 ? (tau < 0 ? 0 : tau) : (int)(pkbf((float)tau, 0.f) & 0xffffu) - (0x4380 - 256);
        kv[e] = tab[idx];
      }
      if (u == 127) kv[16] = 0u;
      char* k0 = sm + S2_K + (2 * ch) * KC + u * 32;
      u32x4 o;
#pragma unroll
      for (int hh = 0; hh < 2; ++hh) {
#pragma unroll
        for (int m = 0; m < 4; ++m) o[m] = kv[8 * hh + 2 * m] | (kv[8 * hh + 2 * m + 1] << 16);
        *(u32x4*)(k0 + hh * 16) = o;
#pragma unroll
        for (int m = 0; m < 4; ++m) o[m] = kv[8 * hh + 2 * m + 1] | (kv[8 * hh + 2 * m + 2] << 16);
        *(u32x4*)(k0 + KC + hh * 16) = o;
      }
      if (u < 4) *(u32x4*)(sm + S2_K + (2 * ch + (u >> 1)) * KC + 4096 + (u & 1) * 16) = u32x4{0u, 0u, 0u, 0u};
    }
    __syncthreads();
    {
      const int ch = w >> 1, r = lane & 15, g = lane >> 4;
      f32x4 acc[8];
#pragma unroll
      for (int a = 0; a < 8; ++a) acc[a] = f32x4{0.f, 0.f, 0.f, 0.f};
      const int s0 = 2031 - 1792 + 8 * g - r - 128 * (w & 1);
      const char* ka = sm + S2_K + (2 * ch + (s0 & 1)) * KC + (s0 >> 1) * 4;
      const char* hb = sm + S2_H + ch * HB + (256 - 16 + 16 * r + 8 * g - 128 * (w & 1)) * 2;
#pragma unroll
      for (int rr = 0; rr < 4; ++rr) {
        u32x4 A[8], B[8];
#pragma unroll
        for (int j = 0; j < 8; ++j) {
          const u32* p = (const u32*)(ka + (7 - j) * 512 - rr * 64);
          A[j] = u32x4{p[0], p[1], p[2], p[3]};
          B[j] = *(const u32x4*)(hb + j * 512 - rr * 64);
        }
#pragma unroll
        for (int j = 0; j < 8; ++j)
#pragma unroll
          for (int v = 0; v + j < 8; ++v) acc[j + v] = mfma16(A[j], B[v], acc[j + v]);
      }
      float* red = (float*)(sm + S2_R) + w * 2048 + 16 * r + 4 * g;
#pragma unroll
      for (int a = 0; a < 8; ++a) *(f32x4*)(red + 256 * a) = acc[a];
    }
    __syncthreads();
#pragma unroll
    for (int c = 0; c < 2; ++c) {
      const float* r0 = (const float*)(sm + S2_R) + (2 * c) * 2048 + 8 * t;
      const f32x4 a0 = *(const f32x4*)r0, a1 = *(const f32x4*)(r0 + 4);
      const f32x4 b0 = *(const f32x4*)(r0 + 2048), b1 = *(const f32x4*)(r0 + 2052);
      const float ds = c ? bhi(dsk) : blo(dsk);
      u32x4 o;
#pragma unroll
      for (int j = 0; j < 4; ++j) {
        const float y0 = j < 2 ? a0[2 * j] + b0[2 * j] : a1[2 * j - 4] + b1[2 * j - 4];
        const float y1 = j < 2 ? a0[2 * j + 1] + b0[2 * j + 1] : a1[2 * j - 3] + b1[2 * j - 3];
        const u32 yb = pkbf(y0, y1);
        const u32 sk = pkbf(blo(hv[c][j]) * ds, bhi(hv[c][j]) * ds);
        u32 p = pkbf(blo(yb) + blo(sk), bhi(yb) + bhi(sk));
        p = pkbf(blo(p) * ga[c], bhi(p) * ga[c]);
        o[j] = pkbf(blo(xv[c][j]) + blo(p), bhi(xv[c][j]) + bhi(p));
      }
      const int d = d0 + c;
      const int off = (t >> 4) * 131072 + (d >> 3) * 2048 + ((t >> 2) & 3) * 512 + (d & 7) * 64 + (((t & 3) ^ ((d >> 2) & 3)) * 16);
      *(u32x4*)(ws + OFF_X1 + off) = o;
    }
    return;
  }
  b -= K2_CONV;
  const int kg = lane >> 3, cg = lane & 7;
  if (b < K2_WP) {
    const int isup = b & 1, kh = (b >> 1) & 1, cblk = b >> 2;
    const u16* W = isup ? wu : wg;
    const int k0 = kh * 256 + w * 64 + kg * 8, f0 = cblk * 64 + cg * 8;
    u32x4 v[8];
#pragma unroll
    for (int i = 0; i < 8; ++i) v[i] = *(const u32x4*)(W + (size_t)(k0 + i) * F_ + f0);
    const f32x4 sb0 = *(const f32x4*)(mod + 1536 + k0), sb1 = *(const f32x4*)(mod + 1536 + k0 + 4);
    const f32x4 cb0 = *(const f32x4*)(mod + 2048 + k0), cb1 = *(const f32x4*)(mod + 2048 + k0 + 4);
    float c1[8], sb[8];
#pragma unroll
    for (int i = 0; i < 4; ++i) {
      c1[i] = rbf(1.f + cb0[i]); c1[4 + i] = rbf(1.f + cb1[i]);
      sb[i] = sb0[i]; sb[4 + i] = sb1[i];
    }
    char* dst = ws + OFF_WP + ((size_t)(f0 >> 4) * WKS + (k0 >> 4)) * 1024 + (((k0 >> 3) & 1) * 32 + isup * 16 + (f0 & 15)) * 16;
    float* red = (float*)sm;
    float s1[8], s2[8];
#pragma unroll
    for (int j = 0; j < 8; ++j) {
      u32x4 o;
      float a1 = 0.f, a2 = 0.f;
#pragma unroll
      for (int m = 0; m < 4; ++m) {
        const float e0 = (j & 1) ? bhi(v[2 * m][j >> 1]) : blo(v[2 * m][j >> 1]);
        const float e1 = (j & 1) ? bhi(v[2 * m + 1][j >> 1]) : blo(v[2 * m + 1][j >> 1]);
        o[m] = pkbf(e0 * c1[2 * m], e1 * c1[2 * m + 1]);
        a1 = dot2bf(o[m], 0x3f803f80u, a1);
        a2 += e0 * sb[2 * m] + e1 * sb[2 * m + 1];
      }
      *(u32x4*)(dst + j * 16) = o;
      s1[j] = a1; s2[j] = a2;
    }
    {
      float* rp = red + (w * 8 + kg) * 132 + cg * 16;
      *(f32x4*)(rp) = f32x4{s1[0], s2[0], s1[1], s2[1]};
      *(f32x4*)(rp + 4) = f32x4{s1[2], s2[2], s1[3], s2[3]};
      *(f32x4*)(rp + 8) = f32x4{s1[4], s2[4], s1[5], s2[5]};
      *(f32x4*)(rp + 12) = f32x4{s1[6], s2[6], s1[7], s2[7]};
    }
    __syncthreads();
    if (t < 64) {
      float a1 = 0.f, a2 = 0.f;
#pragma unroll
      for (int i = 0; i < 32; ++i) {
        const f32x2 p = *(const f32x2*)(red + i * 132 + 2 * t);
        a1 += p[0]; a2 += p[1];
      }
      const float h1 = rbf(a1), m1 = rbf(a1 - h1), l1 = a1 - h1 - m1;
      const float h2 = rbf(a2), m2 = rbf(a2 - h2), l2 = a2 - h2 - m2;
      const int f = cblk * 64 + t;
      const u32x4 o = {pkbf(h1, m1), pkbf(l1, h2), pkbf(m2, l2), 0u};
      *(u32x4*)(ws + OFF_WP + ((size_t)(f >> 4) * WKS + 32) * 1024 + (kh * 32 + isup * 16 + (f & 15)) * 16) = o;
    }
    return;
  }
  b -= K2_WP;
  {
    const int u = b * 4 + w;
    const int fb = u >> 3, db = u & 7;
    const int ks = fb * 4 + (kg >> 1), hh = kg & 1, dd = db * 64 + cg * 8;
    u32x4 v[8];
#pragma unroll
    for (int i = 0; i < 8; ++i) v[i] = *(const u32x4*)(wd + (size_t)(16 * ks + 8 * (i >> 2) + 4 * hh + (i & 3)) * D_ + dd);
    char* dst = ws + OFF_WD + ((size_t)(dd >> 5) * NSLAB + ks) * 1024 + (hh * 32 + (dd & 31)) * 16;
#pragma unroll
    for (int j = 0; j < 8; ++j) {
      u32x4 o;
#pragma unroll
      for (int m = 0; m < 4; ++m)
        o[m] = (j & 1) ? perm_hi(v[2 * m + 1][j >> 1], v[2 * m][j >> 1]) : perm_lo(v[2 * m + 1][j >> 1], v[2 * m][j >> 1]);
      *(u32x4*)(dst + j * 16) = o;
    }
  }
}

constexpr int S3_X = 0, S3_ST = 131072, S3_END = S3_ST + 128 * 4;

template <int IMM> DI void gld16(u32x4& dst, const char* sbase, u32 voff) {
  asm volatile("global_load_dwordx4 %0, %1, %2 offset:%3" : "=v"(dst) : "v"(voff), "s"(sbase), "i"(IMM) : "memory");
}
DI void dma16(const char* sbase, u32 voff, u32 lds) {
  u32 keep;
  asm volatile("s_mov_b32 %0, m0\n\ts_mov_b32 m0, %3\n\ts_nop 0\n\tglobal_load_lds_dwordx4 %1, %2\n\ts_mov_b32 m0, %0"
               : "=&s"(keep) : "v"(voff), "s"(sbase), "s"(lds) : "memory");
}
#define BARRIER() do { asm volatile("" ::: "memory"); __builtin_amdgcn_s_barrier(); asm volatile("" ::: "memory"); } while (0)

__global__ __launch_bounds__(512, 2) void k3(char* __restrict__ ws) {
  extern __shared__ __attribute__((aligned(16))) char sm[];
  const int t = threadIdx.x, lane = t & 63;
  const int w = __builtin_amdgcn_readfirstlane(t >> 6);
  const int xcd = blockIdx.x & 7, idx = blockIdx.x >> 3;
  const int rt = (xcd & 1) * 8 + (idx & 7), ct = (xcd >> 1) * 4 + (idx >> 3);
  const int hh = lane >> 5, blk = (lane >> 4) & 1, q = (lane & 15) >> 2, p = lane & 3;
  const char* xr0 = sm + S3_X + 2048 * hh + 64 * q + 16 * ((2 * blk + (p >> 1)) ^ (2 * hh)) + 8 * (p & 1);
  const char* xr1 = sm + S3_X + 2048 * hh + 64 * (4 + q) + 16 * ((2 * blk + (p >> 1)) ^ (2 * hh + 1)) + 8 * (p & 1);
  const u32 voff = lane * 16;
  const char* xsrc = ws + OFF_X1 + (size_t)rt * 131072 + w * 1024;
  const u32 xdst = (u32)(size_t)(LAS char*)sm + S3_X + w * 1024;
  if (w == 7) {
#pragma unroll
    for (int u = 0; u < 16; ++u) dma16(xsrc + u * 8192, voff, xdst + u * 8192);
    float s[4] = {0.f, 0.f, 0.f, 0.f}, ss[4] = {0.f, 0.f, 0.f, 0.f};
#define STATS(k0, k1)                                                                                      \
  for (int k = (k0); k < (k1); ++k) {                                                                      \
    _Pragma("unroll") for (int lt = 0; lt < 4; ++lt) {                                                     \
      const u32x2 a = tr_read(xr0 + k * 4096 + lt * 512), bb = tr_read(xr1 + k * 4096 + lt * 512);         \
      s[lt] = dot2bf(a[0], 0x3f803f80u, s[lt]); ss[lt] = dot2bf(a[0], a[0], ss[lt]);                       \
      s[lt] = dot2bf(a[1], 0x3f803f80u, s[lt]); ss[lt] = dot2bf(a[1], a[1], ss[lt]);                       \
      s[lt] = dot2bf(bb[0], 0x3f803f80u, s[lt]); ss[lt] = dot2bf(bb[0], bb[0], ss[lt]);                    \
      s[lt] = dot2bf(bb[1], 0x3f803f80u, s[lt]); ss[lt] = dot2bf(bb[1], bb[1], ss[lt]);                    \
    }                                                                                                      \
  }
    asm volatile("s_waitcnt vmcnt(15)" ::: "memory"); BARRIER();
    asm volatile("s_waitcnt vmcnt(14)" ::: "memory"); BARRIER();
    asm volatile("s_waitcnt vmcnt(12)" ::: "memory"); BARRIER();
#pragma unroll 2
    STATS(0, 4)
    asm volatile("s_waitcnt vmcnt(8)" ::: "memory"); BARRIER();
#pragma unroll 2
    STATS(4, 12)
    asm volatile("s_waitcnt vmcnt(0)" ::: "memory"); BARRIER();
#pragma unroll 2
    STATS(12, 32)
#undef STATS
    u32 res[4];
#pragma unroll
    for (int lt = 0; lt < 4; ++lt) {
      const float st = s[lt] + __shfl_xor(s[lt], 32), sst = ss[lt] + __shfl_xor(ss[lt], 32);
      const float mean = st * (1.f / D_);
      const float var = rbf(fmaxf(sst * (1.f / D_) - mean * mean, 0.f));
      const float sd = sqrtf(rbf(var + 1e-5f));
      res[lt] = pkbf(-mean, sd);
    }
    *(u32*)(sm + S3_ST + lane * 4) = lane < 32 ? res[0] : res[1];
    *(u32*)(sm + S3_ST + 256 + lane * 4) = lane < 32 ? res[2] : res[3];
    __syncthreads();
    return;
  }
  const int slab = ct * 7 + w;
  const char* wsrc = ws + OFF_WP + (size_t)slab * WKS * 1024;
  u32x4 W[WKS];
#pragma unroll
  for (int u = 0; u < 8; ++u) {
    dma16(xsrc + (2 * u) * 8192, voff, xdst + (2 * u) * 8192);
    gld16<0>(W[4 * u], wsrc + u * 4096, voff);
    gld16<1024>(W[4 * u + 1], wsrc + u * 4096, voff);
    dma16(xsrc + (2 * u + 1) * 8192, voff, xdst + (2 * u + 1) * 8192);
    gld16<2048>(W[4 * u + 2], wsrc + u * 4096, voff);
    gld16<3072>(W[4 * u + 3], wsrc + u * 4096, voff);
  }
  gld16<0>(W[32], wsrc + 32768, voff);
  f32x16 acc[4];
#pragma unroll
  for (int lt = 0; lt < 4; ++lt)
#pragma unroll
    for (int i = 0; i < 16; ++i) acc[lt][i] = 0.f;
#define KSTEPS(k0, k1)                                                                                     \
  _Pragma("unroll") for (int k = (k0); k < (k1); ++k) {                                                    \
    _Pragma("unroll") for (int lt = 0; lt < 4; ++lt) {                                                     \
      const u32x2 a = tr_read(xr0 + k * 4096 + lt * 512), bb = tr_read(xr1 + k * 4096 + lt * 512);         \
      acc[lt] = mfma32(W[k], u32x4{a[0], a[1], bb[0], bb[1]}, acc[lt]);                                    \
    }                                                                                                      \
  }
  asm volatile("s_waitcnt vmcnt(46)" : "+v"(W[0]), "+v"(W[1]) :: "memory"); BARRIER();
  KSTEPS(0, 2)
  asm volatile("s_waitcnt vmcnt(43)" : "+v"(W[2]), "+v"(W[3]) :: "memory"); BARRIER();
  KSTEPS(2, 4)
  asm volatile("s_waitcnt vmcnt(37)" : "+v"(W[4]), "+v"(W[5]), "+v"(W[6]), "+v"(W[7]) :: "memory"); BARRIER();
  KSTEPS(4, 8)
  asm volatile("s_waitcnt vmcnt(25)" : "+v"(W[8]), "+v"(W[9]), "+v"(W[10]), "+v"(W[11]), "+v"(W[12]), "+v"(W[13]),
               "+v"(W[14]), "+v"(W[15]) :: "memory");
  BARRIER();
  KSTEPS(8, 16)
  asm volatile("s_waitcnt vmcnt(0)" : "+v"(W[16]), "+v"(W[17]), "+v"(W[18]), "+v"(W[19]), "+v"(W[20]), "+v"(W[21]),
               "+v"(W[22]), "+v"(W[23]), "+v"(W[24]), "+v"(W[25]), "+v"(W[26]), "+v"(W[27]), "+v"(W[28]), "+v"(W[29]),
               "+v"(W[30]), "+v"(W[31]), "+v"(W[32]) :: "memory");
  BARRIER();
  KSTEPS(16, 32)
#undef KSTEPS
  __syncthreads();
  char* dst = ws + OFF_HH + ((size_t)(rt * 4) * NSLAB + slab) * 1024 + lane * 16;
#pragma unroll
  for (int lt = 0; lt < 4; ++lt) {
    const u32 st = *(const u32*)(sm + S3_ST + (lt * 32 + (lane & 31)) * 4);
    const u32x4 baug = {perm_lo(st, st), st, perm_hi(st, st), 0u};
    acc[lt] = mfma32(W[32], baug, acc[lt]);
    const float rs = __builtin_amdgcn_rcpf(bhi(st));
    float o[8];
#pragma unroll
    for (int i = 0; i < 8; ++i) {
      const float gv = acc[lt][i] * rs, uv = acc[lt][8 + i] * rs;
      const float e = __builtin_amdgcn_exp2f(gv * (gv * gv * -0.1029432397f - 2.302208198f));
      o[i] = gv * uv * __builtin_amdgcn_rcpf(1.f + e);
    }
    *(u32x4*)(dst + (size_t)lt * NSLAB * 1024) = u32x4{pkbf(o[0], o[1]), pkbf(o[2], o[3]), pkbf(o[4], o[5]), pkbf(o[6], o[7])};
  }
}

constexpr int K4_KS = NSLAB / 4;
constexpr int K4_WIN = 12;
constexpr int S4_R = 0, S4_X = 4 * 64 * 272, S4_END = S4_X + 8192;

__global__ __launch_bounds__(256) void k4(char* __restrict__ ws, u16* __restrict__ out) {
  __shared__ __attribute__((aligned(16))) char sm[S4_END];
  const int t = threadIdx.x, lane = t & 63, w = t >> 6;
  const int xcd = blockIdx.x & 7, idx = blockIdx.x >> 3;
  const int rt = (xcd & 1) * 16 + (xcd >> 1) * 4 + (idx & 3), ct = idx >> 2;
  const u32x4* hp = (const u32x4*)(ws + OFF_HH + ((size_t)(2 * rt) * NSLAB + w * K4_KS) * 1024) + lane;
  const u32x4* dp = (const u32x4*)(ws + OFF_WD + ((size_t)(2 * ct) * NSLAB + w * K4_KS) * 1024) + lane;
  u32x4 A[K4_WIN][2], B[K4_WIN][2];
#pragma unroll
  for (int i = 0; i < K4_WIN; ++i) {
    A[i][0] = dp[i * 64]; A[i][1] = dp[(NSLAB + i) * 64];
    B[i][0] = hp[i * 64]; B[i][1] = hp[(NSLAB + i) * 64];
    __builtin_amdgcn_sched_barrier(0);
  }
  const u32x4* xp = (const u32x4*)(ws + OFF_X1 + (size_t)(rt >> 1) * 131072 + (size_t)(ct * 8) * 2048 + (rt & 1) * 1024);
  const u32x4 x0 = xp[(w * 2) * 128 + lane], x1 = xp[(w * 2 + 1) * 128 + lane];
  const float* mod = (const float*)(ws + OFF_MOD);
  const int eg = lane >> 4;
  const f32x4 gb0 = *(const f32x4*)(mod + 2560 + ct * 64 + eg * 8), gb1 = *(const f32x4*)(mod + 2560 + ct * 64 + eg * 8 + 4);
  const f32x4 gb2 = *(const f32x4*)(mod + 2560 + ct * 64 + 32 + eg * 8), gb3 = *(const f32x4*)(mod + 2560 + ct * 64 + 32 + eg * 8 + 4);
  __builtin_amdgcn_sched_barrier(0);
  f32x16 acc[2][2];
#pragma unroll
  for (int a = 0; a < 2; ++a)
#pragma unroll
    for (int c = 0; c < 2; ++c)
#pragma unroll
      for (int i = 0; i < 16; ++i) acc[a][c][i] = 0.f;
#pragma unroll
  for (int i = 0; i < K4_KS; ++i) {
    const int s = i % K4_WIN;
#pragma unroll
    for (int a = 0; a < 2; ++a)
#pragma unroll
      for (int c = 0; c < 2; ++c) acc[a][c] = mfma32(A[s][a], B[s][c], acc[a][c]);
    if (i + K4_WIN < K4_KS) {
      A[s][0] = dp[(i + K4_WIN) * 64]; A[s][1] = dp[(NSLAB + i + K4_WIN) * 64];
      B[s][0] = hp[(i + K4_WIN) * 64]; B[s][1] = hp[(NSLAB + i + K4_WIN) * 64];
    }
    __builtin_amdgcn_sched_barrier(0);
  }
  *(u32x4*)(sm + S4_X + (w * 2) * 1024 + lane * 16) = x0;
  *(u32x4*)(sm + S4_X + (w * 2 + 1) * 1024 + lane * 16) = x1;
  {
    char* rp = sm + S4_R + w * (64 * 272) + (lane & 31) * 272 + (lane >> 5) * 16;
#pragma unroll
    for (int a = 0; a < 2; ++a)
#pragma unroll
      for (int c = 0; c < 2; ++c)
#pragma unroll
        for (int g = 0; g < 4; ++g)
          *(f32x4*)(rp + c * 32 * 272 + a * 128 + g * 32) =
              f32x4{acc[a][c][4 * g], acc[a][c][4 * g + 1], acc[a][c][4 * g + 2], acc[a][c][4 * g + 3]};
  }
  __syncthreads();
  const int l = 16 * w + (lane & 15);
  const int q = (lane & 15) >> 2, p = lane & 3;
#pragma unroll
  for (int hf = 0; hf < 2; ++hf) {
    const int dg = eg + 4 * hf;
    f32x4 s0 = {0.f, 0.f, 0.f, 0.f}, s1 = {0.f, 0.f, 0.f, 0.f};
#pragma unroll
    for (int ww = 0; ww < 4; ++ww) {
      const char* rp = sm + S4_R + ww * (64 * 272) + l * 272 + dg * 32;
      s0 += *(const f32x4*)rp;
      s1 += *(const f32x4*)(rp + 16);
    }
    const int chk = 2 * (w & 1) + (p >> 1);
    const char* xb = sm + S4_X + dg * 1024 + (w >> 1) * 512 + 8 * (p & 1);
    const u32x2 xa = tr_read(xb + q * 64 + 16 * (chk ^ ((2 * dg) & 3)));
    const u32x2 xc = tr_read(xb + (4 + q) * 64 + 16 * (chk ^ ((2 * dg + 1) & 3)));
    const f32x4 g0 = hf ? gb2 : gb0, g1 = hf ? gb3 : gb1;
    u32x4 o;
    u32 pp;
    pp = pkbf(s0[0], s0[1]); pp = pkbf(blo(pp) * g0[0], bhi(pp) * g0[1]); o[0] = pkbf(blo(xa[0]) + blo(pp), bhi(xa[0]) + bhi(pp));
    pp = pkbf(s0[2], s0[3]); pp = pkbf(blo(pp) * g0[2], bhi(pp) * g0[3]); o[1] = pkbf(blo(xa[1]) + blo(pp), bhi(xa[1]) + bhi(pp));
    pp = pkbf(s1[0], s1[1]); pp = pkbf(blo(pp) * g1[0], bhi(pp) * g1[1]); o[2] = pkbf(blo(xc[0]) + blo(pp), bhi(xc[0]) + bhi(pp));
    pp = pkbf(s1[2], s1[3]); pp = pkbf(blo(pp) * g1[2], bhi(pp) * g1[3]); o[3] = pkbf(blo(xc[1]) + blo(pp), bhi(xc[1]) + bhi(pp));
    *(u32x4*)(out + (size_t)(rt * 64 + l) * D_ + ct * 64 + dg * 8) = o;
  }
}

extern "C" void kernel_launch(void* const* d_in, const int* in_sizes, int n_in, void* d_out, int out_size,
                              void* d_ws, size_t ws_size, hipStream_t stream) {
  const u16* x = (const u16*)d_in[0];
  char* ws = (char*)d_ws;
  (void)hipFuncSetAttribute((const void*)k3, hipFuncAttributeMaxDynamicSharedMemorySize, S3_END);
  k1<<<K1_MOD + K1_KGEN + K1_XT, 256, 0, stream>>>(x, (const u16*)d_in[1], (const u16*)d_in[2], (const u16*)d_in[3],
                                                    (const u16*)d_in[4], (const u16*)d_in[5], (const u16*)d_in[11],
                                                    (const u16*)d_in[12], ws);
  k2<<<K2_CONV + K2_WP + K2_WD, 256, 0, stream>>>((const u16*)d_in[6], (const u16*)d_in[8], (const u16*)d_in[9],
                                                   (const u16*)d_in[10], ws);
  k3<<<256, 512, S3_END, stream>>>(ws);
  k4<<<256, 256, 0, stream>>>(ws, (u16*)d_out);
}
```

```cpp
#include <hip/hip_runtime.h>
#include <hip/hip_bf16.h>
#include <stdint.h>

typedef unsigned short u16;
typedef unsigned int u32;
typedef u32 u32x2 __attribute__((ext_vector_type(2)));
typedef u32 u32x4 __attribute__((ext_vector_type(4)));
typedef float f32x2 __attribute__((ext_vector_type(2)));
typedef float f32x4 __attribute__((ext_vector_type(4)));
typedef float f32x16 __attribute__((ext_vector_type(16)));
typedef short s16x4 __attribute__((ext_vector_type(4)));
typedef __bf16 bf16x2 __attribute__((ext_vector_type(2)));
typedef __bf16 bf16x8 __attribute__((ext_vector_type(8)));
typedef _Float16 f16x2 __attribute__((ext_vector_type(2)));
typedef _Float16 f16x8 __attribute__((ext_vector_type(8)));
#define LAS __attribute__((address_space(3)))
#define DI __device__ __forceinline__

constexpr int L_ = 2048, D_ = 512, N_ = 128, F_ = 1792;
constexpr int NSLAB = F_ / 16;
constexpr int WKS = 34;

constexpr size_t OFF_EP = 0;
constexpr size_t OFF_GB = 1024;
constexpr size_t OFF_SG = 16384;
constexpr size_t OFF_MG = 32768;
constexpr size_t OFF_XG = 131072;
constexpr size_t OFF_X1 = OFF_XG + (size_t)256 * L_ * 8;
constexpr size_t OFF_WP = OFF_X1 + (size_t)D_ * L_ * 2;
constexpr size_t OFF_WD = OFF_WP + (size_t)NSLAB * WKS * 1024;
constexpr size_t OFF_HH = OFF_WD + (size_t)16 * NSLAB * 1024;
constexpr size_t OFF_FL = OFF_HH + (size_t)64 * NSLAB * 1024;
constexpr size_t OFF_CN = OFF_FL + 8192;

DI float blo(u32 u) { return __uint_as_float(u << 16); }
DI float bhi(u32 u) { return __uint_as_float(u & 0xffff0000u); }
DI float bf(u16 h) { return __uint_as_float((u32)h << 16); }
DI u32 pkbf(float a, float b) {
  f32x2 v = {a, b};
  return __builtin_bit_cast(u32, __builtin_convertvector(v, bf16x2));
}
DI u32 pkh(float a, float b) {
  f32x2 v = {a, b};
  return __builtin_bit_cast(u32, __builtin_convertvector(v, f16x2));
}
DI float mul1(float a, float b) { float r = a * b; asm("" : "+v"(r)); return r; }
DI float fma1(float a, float b, float c) { float r = fmaf(a, b, c); asm("" : "+v"(r)); return r; }
DI float rbf(float a) { return blo(pkbf(a, a)); }
DI f32x4 rbf4(f32x4 v) { const u32 a = pkbf(v[0], v[1]), b = pkbf(v[2], v[3]); return f32x4{blo(a), bhi(a), blo(b), bhi(b)}; }
DI u32 perm_lo(u32 hi, u32 lo) { return __builtin_amdgcn_perm(hi, lo, 0x05040100u); }
DI u32 perm_hi(u32 hi, u32 lo) { return __builtin_amdgcn_perm(hi, lo, 0x07060302u); }
DI f32x4 mfma16(u32x4 a, u32x4 b, f32x4 c) {
  return __builtin_amdgcn_mfma_f32_16x16x32_bf16(__builtin_bit_cast(bf16x8, a), __builtin_bit_cast(bf16x8, b), c, 0, 0, 0);
}
DI f32x4 mfma16h(u32x4 a, u32x4 b, f32x4 c) {
  return __builtin_amdgcn_mfma_f32_16x16x32_f16(__builtin_bit_cast(f16x8, a), __builtin_bit_cast(f16x8, b), c, 0, 0, 0);
}
DI f32x16 mfma32(u32x4 a, u32x4 b, f32x16 c) {
  return __builtin_amdgcn_mfma_f32_32x32x16_bf16(__builtin_bit_cast(bf16x8, a), __builtin_bit_cast(bf16x8, b), c, 0, 0, 0);
}
DI u32x2 tr_read(const char* p) {
  return __builtin_bit_cast(u32x2, __builtin_amdgcn_ds_read_tr16_b64_v4i16((LAS s16x4*)p));
}
DI u32x2 tr_readl(u32 a) {
  return __builtin_bit_cast(u32x2, __builtin_amdgcn_ds_read_tr16_b64_v4i16((LAS s16x4*)(size_t)a));
}
DI float dot2bf(u32 a, u32 b, float c) {
  return __builtin_amdgcn_fdot2_f32_bf16(__builtin_bit_cast(bf16x2, a), __builtin_bit_cast(bf16x2, b), c, false);
}
template <int CTRL> DI float dpp_get(float v) {
  return __builtin_bit_cast(float, __builtin_amdgcn_update_dpp(0, __builtin_bit_cast(int, v), CTRL, 0xf, 0xf, true));
}
DI float row_sum(float v) {
  v += dpp_get<0x128>(v); v += dpp_get<0x124>(v); v += dpp_get<0x122>(v); v += dpp_get<0x121>(v);
  return v;
}
DI float rdlane(float v, int l) { return __builtin_bit_cast(float, __builtin_amdgcn_readlane(__builtin_bit_cast(int, v), l)); }
DI float wave_max_nonneg(float v) {
  v = fmaxf(v, dpp_get<0x128>(v)); v = fmaxf(v, dpp_get<0x124>(v));
  v = fmaxf(v, dpp_get<0x122>(v)); v = fmaxf(v, dpp_get<0x121>(v));
  return fmaxf(fmaxf(rdlane(v, 0), rdlane(v, 16)), fmaxf(rdlane(v, 32), rdlane(v, 48)));
}
DI float fexp(float x) { return __builtin_amdgcn_exp2f(x * 1.44269504088896f); }
DI f32x2 rot(f32x2 b) { return f32x2{-b[1], b[0]}; }
DI f32x2 cmul(f32x2 a, f32x2 b, f32x2 bp) {
  return __builtin_elementwise_fma(f32x2{a[1], a[1]}, bp, f32x2{a[0], a[0]} * b);
}
DI f32x2 cexp_p(float zr2, float zi2, float p) {
  const float e = __builtin_amdgcn_exp2f(zr2 * p);
  const float a = __builtin_amdgcn_fractf(zi2 * p);
  return f32x2{e * __builtin_amdgcn_cosf(a), e * __builtin_amdgcn_sinf(a)};
}
template <int IMM> DI void gld16(u32x4& dst, const char* sbase, u32 voff) {
  asm volatile("global_load_dwordx4 %0, %1, %2 offset:%3" : "=v"(dst) : "v"(voff), "s"(sbase), "i"(IMM) : "memory");
}
template <int IMM> DI void gld16c(u32x4& dst, const char* sbase, u32 voff) {
  asm volatile("global_load_dwordx4 %0, %1, %2 offset:%3 sc1" : "=v"(dst) : "v"(voff), "s"(sbase), "i"(IMM) : "memory");
}
template <int IMM> DI void dma16(const char* sbase, u32 voff, u32 lds) {
  asm volatile("s_mov_b32 m0, %2\n\ts_nop 0\n\tglobal_load_lds_dwordx4 %0, %1 offset:%3"
               :: "v"(voff), "s"(sbase), "s"(lds), "i"(IMM) : "memory");
}
DI void gld16w(u32x4& dst, const char* sbase, u32 voff) {
  asm volatile("global_load_dwordx4 %0, %1, %2 nt" : "=v"(dst) : "v"(voff), "s"(sbase) : "memory");
}
template <int IMM> DI void gld8(u32x2& dst, const char* sbase, u32 voff) {
  asm volatile("global_load_dwordx2 %0, %1, %2 offset:%3" : "=v"(dst) : "v"(voff), "s"(sbase), "i"(IMM) : "memory");
}
template <int IMM> DI void gst16s(char* sbase, u32 voff, u32x4 v) {
  asm volatile("global_store_dwordx4 %0, %1, %2 offset:%3 sc1\n\ts_nop 1" :: "v"(voff), "v"(v), "s"(sbase), "i"(IMM) : "memory");
}
DI void gst4s(char* sbase, u32 voff, u32 v) {
  asm volatile("global_store_dword %0, %1, %2 sc1" :: "v"(voff), "v"(v), "s"(sbase) : "memory");
}
#define CODE_TOUCH(dst, FROM, END, off) do {                                                                  \
    const char* pc_; int d0_, d1_;                                                                            \
    asm volatile("s_getpc_b64 %0\n.Lpc%=:\n\ts_mov_b32 %1, " FROM "-.Lpc%=\n\ts_mov_b32 %2, " END "-" FROM   \
                 : "=s"(pc_), "=s"(d0_), "=s"(d1_));                                                          \
    const u32 vo_ = min((u32)((off) + (threadIdx.x & 63) * 128), (u32)(d1_ - 4));                             \
    asm volatile("global_load_dword %0, %1, %2" : "=v"(dst) : "v"(vo_), "s"(pc_ + d0_) : "memory");           \
  } while (0)
#define BARRIER() do { asm volatile("" ::: "memory"); __builtin_amdgcn_s_barrier(); asm volatile("" ::: "memory"); } while (0)
#define SCHED() __builtin_amdgcn_sched_barrier(0)
#define FENCE() asm volatile("" ::: "memory")
template <int N> DI void wait_vm() { asm volatile("s_waitcnt vmcnt(%0)" :: "i"(N) : "memory"); }
template <int N> DI void wait_vm2(u32x4& a, u32x4& b) { asm volatile("s_waitcnt vmcnt(%2)" : "+v"(a), "+v"(b) : "i"(N) : "memory"); }

constexpr int SPIN_CAP = 1 << 16;
constexpr int UROW = 272, UWAVE = 61 * 256;
constexpr int HB = 2304 * 2;
constexpr int KC = 2080 * 2;
constexpr int PW = 3 * 2048 + 1024;
constexpr int RW = 8 * 1280;
constexpr int S2_H = 0, S2_K = 2 * HB, S2_C = S2_K + 4 * KC, S2_M = S2_C + 16384, S2_P = S2_M + 2048,
              S2_U = S2_P + 4 * PW, S2_END = S2_U + 4 * UWAVE;
constexpr int S2_R = S2_U;
DI float add1(float a, float b) { float r = a + b; asm("" : "+v"(r)); return r; }
template <int CTRL, int RM> DI float dpp_or0(float v) {
  return __builtin_bit_cast(float, __builtin_amdgcn_update_dpp(0, __builtin_bit_cast(int, v), CTRL, RM, 0xf, false));
}
DI u32 tag_diff(u32x4 v, u32 tag) { return (v[1] ^ tag) | (v[3] ^ tag); }
DI u32 lds_arrive(u32 addr) {
  u32 arr;
  unsigned long long keep;
  asm volatile("s_mov_b64 %1, exec\n\ts_mov_b64 exec, 1\n\ts_waitcnt lgkmcnt(0)\n\tds_add_rtn_u32 %0, %2, %3\n\ts_waitcnt lgkmcnt(0)\n\ts_mov_b64 exec, %1"
               : "=&v"(arr), "=&s"(keep) : "v"(addr), "v"(1u) : "memory");
  return __builtin_amdgcn_readfirstlane(arr);
}
DI void gst16m(unsigned long long mask, char* sbase, u32 voff, u32x4 v) {
  unsigned long long keep;
  asm volatile("s_mov_b64 %0, exec\n\ts_mov_b64 exec, %4\n\tglobal_store_dwordx4 %1, %2, %3 sc1\n\ts_nop 1\n\ts_mov_b64 exec, %0"
               : "=&s"(keep) : "v"(voff), "v"(v), "s"(sbase), "s"(mask) : "memory");
}
#define LDS4(a) (*(LAS u32x4*)(size_t)(a))
#define FRAG(a, o) __builtin_shufflevector(tr_readl(a), tr_readl((a) + (o)), 0, 1, 2, 3)

extern "C" __global__ __launch_bounds__(512, 2) void adaln_k12(
    const u16* __restrict__ x, const u16* __restrict__ logA, const u16* __restrict__ Aim, const u16* __restrict__ Cre,
    const u16* __restrict__ Cim, const u16* __restrict__ log_dt, const u16* __restrict__ dskip, const u16* __restrict__ wg,
    const u16* __restrict__ wu, const u16* __restrict__ wd, const u16* __restrict__ cvec, const u16* __restrict__ wmod,
    char* __restrict__ ws) {
  extern __shared__ __attribute__((aligned(16))) char sm[];
  const int t = threadIdx.x, lane = t & 63;
  const int w = __builtin_amdgcn_readfirstlane(t >> 6);
  const int b = blockIdx.x;
  const int qt = w & 3, d0 = 2 * b;
  const u32 lbase = (u32)(size_t)(LAS char*)sm;
  u32 pf0 = 0;
  if (b < 16 && w < 2) CODE_TOUCH(pf0, "adaln_k12", "adaln_k34", w * 8192);
  const int rg = lane >> 3, cg = lane & 7;
  const int l0 = 64 * (b >> 3) + 32 * (qt >> 1) + 2 * (qt & 1);
  const int k0 = (b & 3) * 128 + qt * 32;
  u32x4 xi[4];
  u32 pi[6], ci[3];
  u32 vp = (d0 * N_ + t) * 2, vc = (k0 + (lane & 31)) * 2;
  u32 vs = (8 * b + 2 * qt + (lane >> 5)) * (D_ * 2) + (lane & 31) * 16, vt = (l0 + 4 * rg) * (D_ * 2) + (b & 7) * 128 + cg * 16;
  u32 vb = b * 4;
  asm volatile("" : "+v"(vp), "+v"(vc), "+v"(vs), "+v"(vt), "+v"(vb));
  asm volatile("" :: "s"(x), "s"(logA), "s"(Aim), "s"(Cre), "s"(Cim), "s"(log_dt), "s"(dskip), "s"(wg), "s"(wu), "s"(wd),
               "s"(cvec), "s"(wmod), "s"(ws));
  if (w < 4)
    asm volatile("global_load_ushort %0, %6, %8\n\tglobal_load_ushort %1, %6, %9\n\tglobal_load_dword %4, %7, %12\n\t"
                 "global_load_ushort %2, %6, %10\n\tglobal_load_ushort %3, %6, %11\n\tglobal_load_dword %5, %7, %13"
                 : "=&v"(pi[0]), "=&v"(pi[1]), "=&v"(pi[2]), "=&v"(pi[3]), "=&v"(pi[4]), "=&v"(pi[5])
                 : "v"(vp), "v"(vb), "s"(logA), "s"(Aim), "s"(Cre), "s"(Cim), "s"(log_dt), "s"(dskip) : "memory");
  if (w >= 4) {
    asm volatile("global_load_dword %0, %1, %2" : "=v"(ci[1]) : "v"(0u), "s"(ws + OFF_EP) : "memory");
    gld16<0>(xi[2], (const char*)x, vt); gld16<1024>(xi[3], (const char*)x, vt);
    gld16<0>(xi[0], (const char*)x, vs); gld16<512>(xi[1], (const char*)x, vs);
    asm volatile("global_load_ushort %0, %2, %4\n\tglobal_load_dword %1, %3, %5"
                 : "=&v"(ci[0]), "=&v"(ci[2]) : "v"(vc), "v"(vb), "s"(cvec), "s"(dskip) : "memory");
  }
  u32 tag, dsk;
  const int slab0 = ((b & 7) >> 1) * 28 + ((b >> 3) % 7) * 4, kb = (b & 1) * 4 + (b >> 3) / 7;
  const int h = lane >> 5, blk = (lane >> 4) & 1, q = (lane & 15) >> 2, p = lane & 3, r = lane & 15, g = lane >> 4;
  u32 xo = (t >> 5) * 131072 + ((t >> 3) & 3) * 512 + ((((t >> 1) & 3) ^ ((d0 >> 2) & 3)) * 16) + (t & 1) * 64;
  u32 ro = lbase + S2_R + (t >> 6) * 1280 + (t & 3) * 320 + ((t >> 2) & 15) * 16;
  const char* xgp = ws + OFF_XG + (size_t)b * 16384 + w * 2048;
  const char* sgp = ws + OFF_SG + w * 2048;
  f32x4 sk[2], xr[2], ck[2];
#define GATHER() do { gld16c<0>(xg[0], xgp, lane * 16); gld16c<1024>(xg[1], xgp, lane * 16);                  \
                      gld16c<0>(sg[0], sgp, lane * 16); gld16c<1024>(sg[1], sgp, lane * 16); } while (0)
#define NORMALISE() do {                                                                                      \
    asm volatile("" : "+v"(xg[0]), "+v"(xg[1]), "+v"(sg[0]), "+v"(sg[1]));                                    \
    _Pragma("unroll 1") for (int spin = 0; spin < SPIN_CAP; ++spin) {                                         \
      if (__builtin_amdgcn_ballot_w64((tag_diff(xg[0], tag) | tag_diff(xg[1], tag) | tag_diff(sg[0], tag) | tag_diff(sg[1], tag)) != 0u) == 0ull) break; \
      __builtin_amdgcn_s_sleep(4);                                                                            \
      GATHER();                                                                                               \
      asm volatile("s_waitcnt vmcnt(0)" : "+v"(xg[0]), "+v"(xg[1]), "+v"(sg[0]), "+v"(sg[1]) :: "memory");    \
    }                                                                                                         \
    f32x4 mu_, rs_;                                                                                           \
    _Pragma("unroll") for (int i = 0; i < 4; ++i) {                                                           \
      const u32 xs = xg[i >> 1][2 * (i & 1)], st = sg[i >> 1][2 * (i & 1)];                                   \
      xr[0][i] = blo(xs); xr[1][i] = bhi(xs); mu_[i] = blo(st); rs_[i] = __builtin_amdgcn_rcpf(bhi(st));      \
    }                                                                                                         \
    _Pragma("unroll") for (int c = 0; c < 2; ++c) {                                                           \
      const f32x4 a = (xr[c] - mu_) * rs_;                                                                    \
      xn[c] = u32x2{pkbf(a[0], a[1]), pkbf(a[2], a[3])};                                                      \
      *(u32x2*)(sm + S2_H + c * HB + 512 + t * 8) = xn[c];                                                    \
    } } while (0)
#define SKIP() do { _Pragma("unroll") for (int c = 0; c < 2; ++c) { const float ds = c ? bhi(dsk) : blo(dsk); \
      sk[c] = f32x4{mul1(blo(xn[c][0]), ds), mul1(bhi(xn[c][0]), ds), mul1(blo(xn[c][1]), ds), mul1(bhi(xn[c][1]), ds)}; \
      const float ko = w >= 4 ? *(const float*)(sm + S2_M + 20 + c * 8) : 0.f;     \
      const f32x4 kk = *(const f32x4*)(sm + S2_C + c * 8192 + t * 16);                                        \
      ck[c] = f32x4{add1(kk[0], ko), add1(kk[1], ko), add1(kk[2], ko), add1(kk[3], ko)}; }                    \
    asm volatile("" : "+v"(sk[0]), "+v"(sk[1]), "+v"(xr[0]), "+v"(xr[1]), "+v"(xo), "+v"(ro), "+v"(ck[0]), "+v"(ck[1])); } while (0)
  if (w >= 4) {
    const bool prep = b < 224;
    const int ks1 = kb * 4 + qt, ks2 = b >> 1, db = (b & 1) * 4 + qt;
    const u32 stg = lbase + S2_P + qt * PW;
    const u32 ms = lbase + S2_M + 64 + qt * 64;
    const int i5 = lane & 31, j5 = 4 * (i5 >> 3) + (i5 & 3);
    const bool diag = i5 < 16 && ((i5 >> 2) & 1) == h;
    u32x4 mk;
#pragma unroll
    for (int e = 0; e < 4; ++e) mk[e] = i5 == 20 ? 0xffffffffu : diag && (j5 >> 1) == e ? ((j5 & 1) ? 0xffff0000u : 0xffffu) : 0u;
    u32 vmv = (p * 3072 + (lane < 32 ? 2048 : 1536) + ks1 * 16 + ((lane >> 2) & 7) * 2) * 8;
    u32 vcm = (p * 3072 + (q == 3 ? 5 : q) * 512 + d0) * 8;
    u32 aw = stg + lane * 16, am = stg + (8 * h + q) * 128 + blk * 32 + p * 8, ad = stg + 4096 + (4 * h + q) * 128 + blk * 32 + p * 8;
    u32 ag = stg + blk * 2048 + (8 * h + q) * 128 + p * 8, ak = ms + (i5 == 20 ? 32 : 0) + h * 16;
    u32 vw = rg * (F_ * 2) + cg * 16, vd = rg * (D_ * 2) + cg * 16;
    asm volatile("" : "+v"(mk), "+v"(vmv), "+v"(vcm), "+v"(aw), "+v"(am), "+v"(ad),
                 "+v"(ag), "+v"(ak), "+v"(vw), "+v"(vd));
    char* xgs = ws + OFF_XG + (size_t)(b & 7) * 32 * 16384 + (l0 >> 8) * 2048 + (qt & 1) * 1024 + ((l0 & 255) >> 2) * 16;
    char* sgs = ws + OFF_SG + (b >> 5) * 2048 + (qt & 1) * 1024 + (((8 * b + 2 * qt) & 255) >> 2) * 16;
    const char* mrow = (const char*)wmod + ((size_t)k0 * 3072 + (b >> 2) * 64) * 2;
    u32 vx[4], vm[4];
#pragma unroll
    for (int j = 0; j < 4; ++j) { vx[j] = cg * 65536 + rg * 16 + j * 16384; vm[j] = (rg + 8 * j) * 6144 + cg * 16; }
    asm volatile("" : "+s"(xgs), "+s"(sgs), "+s"(mrow), "+v"(vx[0]), "+v"(vx[1]), "+v"(vx[2]), "+v"(vx[3]), "+v"(vm[0]), "+v"(vm[1]),
                 "+v"(vm[2]), "+v"(vm[3]));
    __builtin_amdgcn_s_setprio(2);
    asm volatile("s_waitcnt vmcnt(4)" : "+v"(xi[2]), "+v"(xi[3]), "+v"(ci[1]) :: "memory");
    tag = __builtin_amdgcn_readfirstlane(ci[1]) + 1u;
    {
#pragma unroll
      for (int j = 0; j < 4; ++j) gst16s<0>(xgs, vx[j], u32x4{xi[2][j], tag, xi[3][j], tag});
      asm volatile("s_waitcnt vmcnt(4)" : "+v"(xi[0]), "+v"(xi[1]), "+v"(ci[0]), "+v"(ci[2]) :: "memory");
      dsk = __builtin_amdgcn_readfirstlane(ci[2]);
      float s0 = 0.f, s1 = 0.f, q0 = 0.f, q1 = 0.f;
#pragma unroll
      for (int j = 0; j < 4; ++j) {
        s0 = dot2bf(xi[0][j], 0x3f803f80u, s0); q0 = dot2bf(xi[0][j], xi[0][j], q0);
        s1 = dot2bf(xi[1][j], 0x3f803f80u, s1); q1 = dot2bf(xi[1][j], xi[1][j], q1);
      }
      float sum = row_sum(add1(s0, s1));
      asm("" : "+v"(sum));
      float sq = row_sum(add1(q0, q1));
      sum = add1(sum, dpp_or0<0x142, 0xa>(sum)); sq = add1(sq, dpp_or0<0x142, 0xa>(sq));
      const float mean = sum * (1.f / D_);
      const float var = rbf(fmaxf(sq * (1.f / D_) - mean * mean, 0.f));
      const u32 pk = pkbf(mean, __builtin_amdgcn_sqrtf(rbf(var + 1e-5f)));
      const auto pr = __builtin_amdgcn_permlane32_swap(pk, pk, false, false);
      gst16m(1ull << 16, sgs, 0u, u32x4{pr[0], tag, pr[1], tag});
    }
    u32x4 wv[4];
    if (b < 192) {
#pragma unroll
      for (int i = 0; i < 4; ++i) gld16w(wv[i], mrow, vm[i]);
    }
    __builtin_amdgcn_s_setprio(0);
    u32x4 sc[2];
    {
      const float c = blo(ci[0]);
      *(u32*)(sm + S2_M + 32) = tag;
      *(LAS u16*)(size_t)(ms + (lane & 31) * 2) = (u16)pkbf(c * __builtin_amdgcn_rcpf(1.f + fexp(-c)), 0.f);
      FENCE();
      sc[0] = LDS4(ms + h * 16); sc[1] = LDS4(ms + 32 + h * 16);
    }
    const u32x4 zz = {0u, 0u, 0u, 0u};
    u32x4 s0a = i5 == 0 ? sc[0] : zz, s0b = i5 == 0 ? sc[1] : zz, s4a = i5 == 4 ? sc[0] : zz, s4b = i5 == 4 ? sc[1] : zz;
    asm volatile("" : "+v"(s0a), "+v"(s0b), "+v"(s4a), "+v"(s4b));
    f32x16 z;
#pragma unroll
    for (int i = 0; i < 16; ++i) z[i] = 0.f;
    if (b < 192) wait_vm<4>(); else wait_vm<0>();
    asm volatile("s_waitcnt lgkmcnt(0)" ::: "memory");
    BARRIER();
    u32x4 xg[2], sg[2], raw[6];
    u32x2 xn[2];
    bool published = false;
    if (b < 192) {
      asm volatile("s_waitcnt vmcnt(0)" : "+v"(wv[0]), "+v"(wv[1]), "+v"(wv[2]), "+v"(wv[3]) :: "memory");
#pragma unroll
      for (int i = 0; i < 4; ++i) LDS4(aw + i * 1024) = wv[i];
      FENCE();
      const f32x16 m0 = mfma32(s0b, FRAG(am + 2048, 512), mfma32(s0a, FRAG(am, 512), z));
      const f32x16 m1 = mfma32(s4b, FRAG(am + 2048 + 64, 512), mfma32(s4a, FRAG(am + 64, 512), z));
      *(LAS float*)(size_t)(lbase + S2_M + 1024 + qt * 256 + lane * 4) = m0[0] + m1[0];
    } else {
      __builtin_amdgcn_s_sleep(8);
    }
    GATHER();
    if (b < 192) {
      if (lds_arrive(lbase + S2_M + 48) == 3u) {
        const char* rp = sm + S2_M + 1024 + (lane & 31) * 8;
        const f32x2 a = (*(const f32x2*)rp + *(const f32x2*)(rp + 256)) + (*(const f32x2*)(rp + 512) + *(const f32x2*)(rp + 768));
        gst16m(0xffffffffull, ws + OFF_MG + ((b & 3) * 3072 + (b >> 2) * 64) * 8, lane * 16, u32x4{__float_as_uint(a[0]), tag, __float_as_uint(a[1]), tag});
        published = true;
      }
    }
    if (prep) {
      const size_t go = ((size_t)ks1 * 16 * F_ + slab0 * 16) * 2;
      const char* wdp = (const char*)wd + ((size_t)ks2 * 16 * D_ + db * 64) * 2;
      gld16w(raw[4], wdp, vd); gld16w(raw[5], wdp + 8 * D_ * 2, vd);
      gld16w(raw[0], (const char*)wg + go, vw); gld16w(raw[2], (const char*)wu + go, vw);
      gld16w(raw[1], (const char*)wg + go + 8 * F_ * 2, vw); gld16w(raw[3], (const char*)wu + go + 8 * F_ * 2, vw);
    }
    if (published) wait_vm<7>(); else if (prep) wait_vm<6>(); else wait_vm<0>();
    NORMALISE();
    __syncthreads();
    __builtin_amdgcn_s_setprio(2);
    u32x4 mv, cm, fr[6];
    if (b >= 224 && b < 232) {
      u32 pf1;
      CODE_TOUCH(pf1, "adaln_k34", "adaln_end", qt * 8192);
      asm volatile("s_waitcnt vmcnt(0)" :: "v"(pf1) : "memory");
    } else if (!prep) {
      __builtin_amdgcn_s_sleep(16);
    }
    if (prep) {
      asm volatile("s_waitcnt vmcnt(0)" : "+v"(raw[0]), "+v"(raw[1]), "+v"(raw[2]), "+v"(raw[3]), "+v"(raw[4]), "+v"(raw[5]) :: "memory");
#pragma unroll
      for (int i = 0; i < 6; ++i) LDS4(aw + i * 1024) = raw[i];
    }
    gld16c<0>(mv, ws + OFF_MG, vmv); gld16c<0>(cm, ws + OFF_MG, vcm);
    if (prep) {
      fr[4] = FRAG(ad, 1024); fr[5] = FRAG(ad + 64, 1024);
#pragma unroll
      for (int k = 0; k < 4; ++k) fr[k] = FRAG(ag + k * 32, 512);
    }
    SKIP();
    asm volatile("s_waitcnt vmcnt(0)" : "+v"(mv), "+v"(cm) :: "memory");
#pragma unroll 1
    for (int spin = 0; spin < SPIN_CAP; ++spin) {
      if (__builtin_amdgcn_ballot_w64((tag_diff(mv, tag) | tag_diff(cm, tag)) != 0u) == 0ull) break;
      __builtin_amdgcn_s_sleep(1);
      gld16c<0>(mv, ws + OFF_MG, vmv); gld16c<0>(cm, ws + OFF_MG, vcm);
      asm volatile("s_waitcnt vmcnt(0)" : "+v"(mv), "+v"(cm) :: "memory");
    }
#define QSUM(v) do { v = add1(v, dpp_get<0xB1>(v)); v = add1(v, dpp_get<0x4E>(v)); } while (0)
    if (w == 7) {
      float c0 = __uint_as_float(cm[0]), c1 = __uint_as_float(cm[2]);
      QSUM(c0); QSUM(c1);
      const u32 pk = pkbf(c0, c1);
      const u32 sa = __builtin_amdgcn_readlane(pk, 0), ca = __builtin_amdgcn_readlane(pk, 4), ga = __builtin_amdgcn_readlane(pk, 8);
      const u32 gb = __builtin_amdgcn_readlane(pk, 12);
      *(f32x4*)(sm + S2_M) = f32x4{rbf(add1(1.f, blo(ca))), rbf(add1(1.f, bhi(ca))), blo(sa), bhi(sa)};
      *(f32x4*)(sm + S2_M + 512) = f32x4{blo(ga), bhi(ga), mul1(blo(sa), blo(dsk)), mul1(bhi(sa), bhi(dsk))};
      asm volatile("global_store_dwordx2 %0, %1, %2 sc1" :: "v"(0u), "v"(u32x2{gb << 16, gb & 0xffff0000u}), "s"(ws + OFF_GB + d0 * 4) : "memory");
      if (b == 0) gst4s(ws + OFF_EP, 0u, tag);
    }
    if (prep) {
      char* dd = ws + OFF_WD + ((size_t)db * NSLAB + ks2) * 2048;
      gst16s<0>(dd, lane * 16, fr[4]);
      gst16s<1024>(dd, lane * 16, fr[5]);
      float m0 = __uint_as_float(mv[0]), m1 = __uint_as_float(mv[2]);
      QSUM(m0); QSUM(m1);
      const u32 pk = pkbf(m0, m1);
      *(LAS u32*)(size_t)(ms + (lane >> 2) * 4) = lane < 32 ? pkbf(add1(1.f, blo(pk)), add1(1.f, bhi(pk))) : pk;
      FENCE();
      const u32x4 a2 = LDS4(ak) & mk;
      const u32 one = i5 == 16 ? 0x3f803f80u : 0u;
      const u32x4 a1 = {one, one, one, one};
      char* dst = ws + OFF_WP + ((size_t)slab0 * WKS + ks1) * 1024;
      f32x16 dv[4];
#pragma unroll
      for (int k = 0; k < 4; ++k) dv[k] = mfma32(a2, fr[k], z);
      float cs[4];
#pragma unroll
      for (int k = 0; k < 4; ++k) {
        const u32x4 o = {pkbf(dv[k][0], dv[k][1]), pkbf(dv[k][2], dv[k][3]), pkbf(dv[k][4], dv[k][5]), pkbf(dv[k][6], dv[k][7])};
        gst16s<0>(dst + k * (WKS * 1024), lane * 16, o);
        cs[k] = mfma32(a1, o, dv[k])[8];
      }
      *(LAS f32x4*)(size_t)(aw + 6144) = f32x4{cs[0], cs[1], cs[2], cs[3]};
      if (lds_arrive(lbase + S2_M + 52) == 3u) {
        const char* cs = sm + S2_P + 6144 + (lane & 15) * 32 + (lane >> 4) * 4;
        u32x4 o;
#pragma unroll
        for (int i = 0; i < 4; ++i) {
          const char* c0 = cs + (i >> 1) * 16 + (i & 1) * 512;
          const float tot = add1(add1(*(const float*)c0, *(const float*)(c0 + PW)), add1(*(const float*)(c0 + 2 * PW), *(const float*)(c0 + 3 * PW)));
          o[i] = __float_as_uint(tot);
        }
        gst16s<0>(ws + OFF_WP + ((size_t)slab0 * WKS + 32) * 1024 + kb * 256, (lane >> 4) * (WKS * 1024) + (lane & 15) * 16, o);
      }
    }
#undef QSUM
  } else {
    const int u = t & 127;
    u32 gi[17];
#pragma unroll
    for (int e = 0; e < 17; ++e) {
      const int tau = e == 16 ? max(2047 - 16 * u - e, 0) : 2047 - 16 * u - e;
      const int idx = max((int)(pkbf((float)tau, 0.f) & 0xffffu) - (0x4380 - 256), min(tau, 128));
      gi[e] = lbase + S2_U + (w & 2) * UWAVE + (idx + (idx >> 5)) * 4;
      asm volatile("" : "+v"(gi[e]));
    }
    asm volatile("s_waitcnt vmcnt(0)" : "+v"(pi[0]), "+v"(pi[1]), "+v"(pi[2]), "+v"(pi[3]), "+v"(pi[4]), "+v"(pi[5]), "+v"(pf0) :: "memory");
    const u32 ldt = pi[4];
    dsk = __builtin_amdgcn_readfirstlane(pi[5]);
    const float lar = blo(pi[0]), ai = blo(pi[1]), cr = blo(pi[2]), ci = blo(pi[3]);
    const float dt = rbf(fexp((w >> 1) ? bhi(ldt) : blo(ldt)));
    const float ea = rbf(fexp(lar));
    const float zr2 = (-ea * dt) * 1.44269504088896f, zi2 = (ai * dt) * 0.159154943091895f;
    char* base = sm + S2_U + w * UWAVE;
    char* wr = base + lane * 4;
#define PUT(row, v) (*(u32*)(wr + (row) * UROW) = pkh((v)[0], (v)[1]))
    f32x2 E[8], Ep[8];
    E[0] = f32x2{1.f, 0.f};
    E[1] = cexp_p(zr2, zi2, 1.f); Ep[1] = rot(E[1]);
    E[2] = cmul(E[1], E[1], Ep[1]); Ep[2] = rot(E[2]);
    E[3] = cmul(E[2], E[1], Ep[1]);
    E[4] = cmul(E[2], E[2], Ep[2]); Ep[4] = rot(E[4]);
    E[5] = cmul(E[1], E[4], Ep[4]);
    E[6] = cmul(E[2], E[4], Ep[4]); Ep[6] = rot(E[6]);
    E[7] = cmul(E[3], E[4], Ep[4]);
    const f32x2 W8c = cexp_p(zr2, -zi2, 8.f), W8p = rot(W8c);
    const f32x2 W128 = cexp_p(zr2, zi2, 128.f), W128p = rot(W128);
    const float m = wave_max_nonneg(fmaxf(fabsf(cr), fabsf(ci)));
    const int ex = __builtin_amdgcn_frexp_expf(m);
    f32x2 V = {__builtin_amdgcn_ldexpf(cr, -ex), __builtin_amdgcn_ldexpf(-ci, -ex)};
    f32x2 Wc = W128;
    PUT(0, E[0]);
#pragma unroll
    for (int i = 1; i <= 16; ++i) {
      const f32x2 Wn = cmul(Wc, W128, W128p), Vn = cmul(V, W8c, W8p);
      PUT(40 + i, V);
      PUT(i == 1 ? 8 : i < 4 ? 16 + 4 * (i - 2) : i < 8 ? 24 + 2 * (i - 4) : 24 + i, Wc);
      if (i == 2 || i == 3) {
#pragma unroll
        for (int q = 1; q < 4; ++q) { const f32x2 u = cmul(Wc, E[2 * q], Ep[2 * q]); PUT(16 + 4 * (i - 2) + q, u); }
      }
      if (i >= 4 && i < 8) { const f32x2 u = cmul(Wc, E[4], Ep[4]); PUT(24 + 2 * (i - 4) + 1, u); }
      if (i >= 8 && i < 15) { const f32x2 u = cmul(E[i - 7], W128, W128p); PUT(i + 1, u); PUT(i - 7, E[i - 7]); }
      Wc = Wn; V = Vn;
      SCHED();
    }
#undef PUT
    f32x4 ka[3];
    {
      FENCE();
      const char* rd = base + r * UROW + g * 16;
      u32x4 vb[4], ua[4][3];
#pragma unroll
      for (int ks = 0; ks < 4; ++ks) {
        vb[ks] = *(const u32x4*)(rd + 41 * UROW + ks * 64);
#pragma unroll
        for (int mt = 0; mt < 3; ++mt) ua[ks][mt] = *(const u32x4*)(rd + mt * 16 * UROW + ks * 64);
      }
      SCHED();
      *(u32x4*)(sm + (w < 2 ? S2_H + w * HB + (lane & 31) * 16 : w == 2 ? S2_K + ((lane >> 1) & 3) * KC + 4096 + (lane & 1) * 16 : S2_M + 48)) = u32x4{0u, 0u, 0u, 0u};
      if (w == 3) gst16m(15ull, ws + OFF_FL, (lane & 2) ? 8192 + b * 256 + (lane & 1) * 128 : b * 32 + (lane & 1) * 16, u32x4{0u, 0u, 0u, 0u});
#pragma unroll
      for (int mt = 0; mt < 3; ++mt) ka[mt] = f32x4{0.f, 0.f, 0.f, 0.f};
#pragma unroll
      for (int ks = 0; ks < 4; ++ks)
#pragma unroll
        for (int mt = 0; mt < 3; ++mt) ka[mt] = mfma16h(ua[ks][mt], vb[ks], ka[mt]);
    }
    {
      const float fac = __builtin_amdgcn_ldexpf(2.f * dt, ex);
      const int i0 = 128 * (g >> 1) + 4 * (g & 1) + 8 * r, i1 = 256 + 64 * g + (g < 2 ? 4 : 2) * r, i2 = 512 + 64 * g + r;
      float* t0 = (float*)base + i0 + (i0 >> 5);
      float* t1 = (float*)base + i1 + (i1 >> 5);
      float* t1b = t1 + (g < 2 ? 2 : 33);
      float* t2 = (float*)base + i2 + (i2 >> 5);
#pragma unroll
      for (int i = 0; i < 4; ++i) {
        t0[i] = ka[0][i] * fac;
        (i < 2 ? t1 : t1b)[i & 1] = ka[1][i] * fac;
        t2[16 * i + (i >> 1)] = ka[2][i] * fac;
      }
    }
    __syncthreads();
    u32x4 xg[2], sg[2];
    u32x2 xn[2];
    const u32 tagv = *(const u32*)(sm + S2_M + 32);
    float kv[17];
    {
      float k2[17];
#pragma unroll
      for (int e = 0; e < 17; ++e) { kv[e] = *(const LAS float*)(size_t)gi[e]; k2[e] = *(const LAS float*)(size_t)(gi[e] + UWAVE); }
      SCHED();
#pragma unroll
      for (int e = 0; e < 17; ++e) kv[e] = add1(kv[e], k2[e]);
    }
    if (u == 127) kv[16] = 0.f;
    tag = __builtin_amdgcn_readfirstlane(tagv);
    SCHED();
    GATHER();
#pragma unroll
    for (int i = 0; i < 2; ++i) {
      u32x4 o0, o1;
#pragma unroll
      for (int m = 0; m < 4; ++m) { o0[m] = pkbf(kv[8 * i + 2 * m], kv[8 * i + 2 * m + 1]); o1[m] = pkbf(kv[8 * i + 2 * m + 1], kv[8 * i + 2 * m + 2]); }
      *(u32x4*)(sm + S2_K + (w & 2) * KC + u * 32 + i * 16) = o0;
      *(u32x4*)(sm + S2_K + ((w & 2) + 1) * KC + u * 32 + i * 16) = o1;
    }
    lds_arrive(lbase + S2_M + 56 + (w >> 1) * 4);
    {
#pragma unroll
      for (int e = 14; e >= 0; --e) kv[e] += kv[e + 1];
      float inc = kv[0];
      inc += dpp_or0<0x111, 0xf>(inc); inc += dpp_or0<0x112, 0xf>(inc); inc += dpp_or0<0x114, 0xf>(inc); inc += dpp_or0<0x118, 0xf>(inc);
      inc += dpp_or0<0x142, 0xa>(inc); inc += dpp_or0<0x143, 0xc>(inc);
      const float tot = rdlane(inc, 63), off = tot - inc;
      char* cp = sm + S2_C + (w >> 1) * 8192 + (127 - u) * 64;
#pragma unroll
      for (int i = 0; i < 4; ++i)
        *(f32x4*)(cp + i * 16) = f32x4{add1(kv[15 - 4 * i], off), add1(kv[14 - 4 * i], off), add1(kv[13 - 4 * i], off), add1(kv[12 - 4 * i], off)};
      *(float*)(sm + S2_M + 16 + w * 4) = tot;
    }
    const int s0 = 2031 - 1792 - 96 + 8 * g - r - 128 * (w & 1);
    u32 kb4[4], hb = lbase + S2_H + (w >> 1) * HB + (256 - 16 - 96 + 16 * r + 8 * g - 128 * (w & 1)) * 2;
#pragma unroll
    for (int i = 0; i < 4; ++i) kb4[i] = lbase + S2_K + ((w & 2) + (s0 & 1)) * KC + (s0 >> 1) * 4 + i * 1024;
    asm volatile("" : "+v"(kb4[0]), "+v"(kb4[1]), "+v"(kb4[2]), "+v"(kb4[3]), "+v"(hb));
    u32x4 A[2][8], B[2][8];
#define LDA(s, j, rr) do { const LAS u32* p_ = (const LAS u32*)(size_t)(kb4[(7 - (j)) >> 1] + ((7 - (j)) & 1) * 512 + (rr) * 64); \
                           A[s][j] = u32x4{p_[0], p_[1], p_[2], p_[3]}; } while (0)
#define LDB(s, j, rr) (B[s][j] = *(const LAS u32x4*)(size_t)(hb + (j) * 512 + (rr) * 64))
#pragma unroll 1
    for (int spin = 0; spin < SPIN_CAP * 16; ++spin)
      if (__builtin_amdgcn_readfirstlane(*(volatile LAS u32*)(size_t)(lbase + S2_M + 56 + (w >> 1) * 4)) >= 2u) break;
    FENCE();
#pragma unroll
    for (int j = 0; j < 8; ++j) { LDA(0, j, 0); LDA(1, j, 1); }
    SCHED();
    wait_vm<0>();
    NORMALISE();
    __syncthreads();
    f32x4 acc[8];
#pragma unroll
    for (int a = 0; a < 8; ++a) acc[a] = f32x4{0.f, 0.f, 0.f, 0.f};
#pragma unroll
    for (int j = 0; j < 8; ++j) LDB(0, j, 0);
    SCHED();
#pragma unroll
    for (int rr = 0; rr < 4; ++rr) {
      if (rr < 3) {
#pragma unroll
        for (int j = 0; j < 8; ++j) { if (rr > 0) LDA((rr + 1) & 1, j, rr + 1); LDB((rr + 1) & 1, j, rr + 1); }
      }
#pragma unroll
      for (int j = 0; j < 8; ++j)
#pragma unroll
        for (int v = 0; v + j < 8; ++v) acc[j + v] = mfma16(A[rr & 1][j], B[rr & 1][v], acc[j + v]);
      if (rr < 3) {
#pragma unroll
        for (int i = 0; i < (rr > 0 ? 24 : 8); ++i) {
          if (rr > 0) __builtin_amdgcn_sched_group_barrier(0x8, 1, 0); else __builtin_amdgcn_sched_group_barrier(0x8, 3, 0);
          __builtin_amdgcn_sched_group_barrier(0x100, 1, 0);
        }
        __builtin_amdgcn_sched_group_barrier(0x8, 12, 0);
      }
      SCHED();
    }
#undef LDA
#undef LDB
    char* red = sm + S2_R + qt * RW + g * 320 + r * 16;
#pragma unroll
    for (int a = 0; a < 8; ++a) *(f32x4*)(red + 1280 * a) = acc[a];
    SKIP();
  }
#undef GATHER
#undef NORMALISE
#undef SKIP
  __syncthreads();
  {
    char* xd = ws + OFF_X1 + (d0 >> 3) * 2048 + (d0 & 7) * 64;
    const f32x4 ab = *(const f32x4*)(sm + S2_M), gd = *(const f32x4*)(sm + S2_M + 512);
#define RD4(o) (*(const LAS f32x4*)(size_t)(ro + (o)))
    const f32x4 y0 = (RD4(0) + RD4(RW)) * ab[0] + ck[0] * ab[2], y1 = (RD4(2 * RW) + RD4(3 * RW)) * ab[1] + ck[1] * ab[3];
#undef RD4
    const f32x4 o0 = xr[0] + rbf4(rbf4(rbf4(y0) + rbf4(sk[0] * ab[0] + gd[2])) * gd[0]);
    const f32x4 o1 = xr[1] + rbf4(rbf4(rbf4(y1) + rbf4(sk[1] * ab[1] + gd[3])) * gd[1]);
    const u32 a0 = pkbf(o0[0], o0[1]), a1 = pkbf(o0[2], o0[3]), b0 = pkbf(o1[0], o1[1]), b1 = pkbf(o1[2], o1[3]);
    const bool odd = t & 1;
    const u32 r0 = __builtin_amdgcn_mov_dpp(odd ? a0 : b0, 0xB1, 0xf, 0xf, true), r1 = __builtin_amdgcn_mov_dpp(odd ? a1 : b1, 0xB1, 0xf, 0xf, true);
    gst16s<0>(xd, xo, odd ? u32x4{r0, r1, b0, b1} : u32x4{a0, a1, r0, r1});
  }
}

constexpr int S3_ST = 131072, S3_SC = S3_ST + 512, S3_X1 = S3_SC + 8192, S3_CT = S3_X1 + 8192, S3_END = S3_CT + 16;
constexpr int NPRE = 6;
constexpr int NW = 6;

template <bool PK> DI u32x4 geglu(const f32x16& acc, u32 st) {
  const float sd = bhi(st), rs = __builtin_amdgcn_rcpf(sd);
  const float k2c = -2.302208198f * rs, k1c = -0.1029432397f * rs * rs * rs, sd2 = sd * sd;
  u32x4 o;
#pragma unroll
  for (int i = 0; i < 4; ++i) {
    if (PK) {
      const f32x2 ga = {acc[2 * i], acc[2 * i + 1]}, bb = {acc[8 + 2 * i], acc[9 + 2 * i]};
      const f32x2 z = ga * __builtin_elementwise_fma(ga * ga, f32x2{k1c, k1c}, f32x2{k2c, k2c});
      const f32x2 e = {__builtin_amdgcn_exp2f(z[0]), __builtin_amdgcn_exp2f(z[1])};
      const f32x2 dn = __builtin_elementwise_fma(e, f32x2{sd2, sd2}, f32x2{sd2, sd2});
      const f32x2 r = ga * bb * f32x2{__builtin_amdgcn_rcpf(dn[0]), __builtin_amdgcn_rcpf(dn[1])};
      o[i] = pkbf(r[0], r[1]);
    } else {
      float r[2];
#pragma unroll
      for (int j = 0; j < 2; ++j) {
        const float ga = acc[2 * i + j];
        const float e = __builtin_amdgcn_exp2f(mul1(ga, fma1(mul1(ga, ga), k1c, k2c)));
        r[j] = mul1(mul1(ga, acc[8 + 2 * i + j]), __builtin_amdgcn_rcpf(fma1(e, sd2, sd2)));
      }
      o[i] = pkbf(r[0], r[1]);
    }
  }
  return o;
}
DI void signal_last(u32 lds, char* flag) {
  unsigned long long keep;
  u32 tmp;
  asm volatile("s_mov_b64 %0, exec\n\ts_mov_b64 exec, 1\n\tds_add_rtn_u32 %1, %2, %3\n\ts_waitcnt lgkmcnt(0)\n\t"
               "v_cmp_eq_u32 vcc, 6, %1\n\ts_and_b64 exec, exec, vcc\n\tglobal_store_dword %4, %3, %5 sc1\n\ts_mov_b64 exec, %0"
               : "=&s"(keep), "=&v"(tmp) : "v"(lds), "v"(1u), "v"(0u), "s"(flag) : "memory", "vcc", "scc");
}
DI void wait_flags(const char* flags) {
  const u32 vo = (threadIdx.x & 15) * 128;
#pragma unroll 1
  for (int spin = 0; spin < SPIN_CAP; ++spin) {
    u32 v;
    asm volatile("global_load_dword %0, %1, %2 sc1\n\ts_waitcnt vmcnt(0)" : "=&v"(v) : "v"(vo), "s"(flags) : "memory");
    if (__builtin_amdgcn_ballot_w64(v == 0u) == 0ull) break;
    __builtin_amdgcn_s_sleep(4);
  }
}

template <int SLOT> struct HReg;
#define HREG(S, Q, LO, HI)                                                                                    \
  template <> struct HReg<S> {                                                                                \
    template <int IMM> static DI void load(u32x4& d, const char* sb, u32 vo) {                                \
      asm volatile("global_load_dwordx4 %0, %1, %2 offset:%3 sc1" : "={" Q "}"(d) : "v"(vo), "s"(sb), "i"(IMM) : "memory"); } \
    template <int N> static DI void wait(u32x4& d) { asm volatile("s_waitcnt vmcnt(%1)" : "+{" Q "}"(d) : "i"(N) : "memory"); } \
    template <int N, int IMM> static DI void wait_checked(u32x4& d, const char* sb, u32 vo) {                 \
      u32 tv, ts;                                                                                             \
      asm volatile("s_waitcnt vmcnt(%5)\n\ts_mov_b32 %2, 0x10000\n"                                          \
                   ".Lchk%=:\n\tv_max_u32 %1, " LO ", " HI "\n\tv_cmp_eq_u32 vcc, -1, %1\n\ts_cbranch_vccz .Lok%=\n\t" \
                   "s_sleep 2\n\tglobal_load_dwordx4 %0, %3, %4 offset:%6 sc1\n\ts_sub_u32 %2, %2, 1\n\t"    \
                   "s_waitcnt vmcnt(0)\n\ts_cbranch_scc0 .Lchk%=\n.Lok%=:"                                    \
                   : "+{" Q "}"(d), "=&v"(tv), "=&s"(ts) : "v"(vo), "s"(sb), "i"(N), "i"(IMM) : "memory", "vcc", "scc"); } \
  };
HREG(0, "v[224:227]", "v224", "v226") HREG(1, "v[228:231]", "v228", "v230") HREG(2, "v[232:235]", "v232", "v234")
HREG(3, "v[236:239]", "v236", "v238") HREG(4, "v[240:243]", "v240", "v242") HREG(5, "v[244:247]", "v244", "v246")
#undef HREG

constexpr int EXC = 1056, EXS = 8 * EXC, EXT = 8 * EXS;
DI u32x2 finish_tile(const f32x4 (&v)[8], f32x4 xres, u32x4 gv) {
  const f32x4 s = ((v[0] + v[1]) + (v[2] + v[3])) + ((v[4] + v[5]) + (v[6] + v[7]));
  const f32x4 o = __builtin_elementwise_fma(s, __builtin_bit_cast(f32x4, gv), xres);
  return u32x2{pkbf(o[0], o[1]), pkbf(o[2], o[3])};
}

extern "C" __global__ __launch_bounds__(512, 2) void adaln_k34(char* __restrict__ ws, u16* __restrict__ out) {
  extern __shared__ __attribute__((aligned(16))) char sm[];
  const int t = threadIdx.x, lane = t & 63;
  const int w = __builtin_amdgcn_readfirstlane(t >> 6);
  const int xcd = blockIdx.x & 7, idx = blockIdx.x >> 3;
  const int rt = (xcd & 1) * 8 + (idx & 7), ct = (xcd >> 1) * 4 + (idx >> 3);
  const int pb = (xcd & 1) * 8 + (xcd >> 1) * 2 + ((idx >> 1) & 1), eb = idx & 1, cb = idx >> 2;
  const int q = (lane & 15) >> 2, p = lane & 3, hh = lane >> 5, blk = (lane >> 4) & 1;
  const u32 voff = lane * 16;
  const u32 lbase = (u32)(size_t)(LAS char*)sm;
  const char* xsrc = ws + OFF_X1 + (size_t)rt * 131072 + w * 2048;
  const u32 xdst = lbase + w * 1024;
  const char* dsrc = ws + OFF_WD + ((size_t)cb * NSLAB + w * 14) * 2048;
  u32x4 A[28], gv;
#define ISSUE_X(hf, m) dma16<0>(xsrc + (m) * 16384 + (hf) * 1024, voff, xdst + (hf) * 65536 + (m) * 8192)
#define X2(s) do { if ((s) == 5) { ISSUE_X(1, 0); ISSUE_X(1, 1); ISSUE_X(1, 2); }                              \
                   if ((s) == 6) { ISSUE_X(1, 3); ISSUE_X(1, 4); ISSUE_X(1, 5); }                              \
                   if ((s) == 7) { ISSUE_X(1, 6); ISSUE_X(1, 7); } } while (0)
#define LOAD_A(i) gld16<((i) & 3) * 1024>(A[i], dsrc + ((i) >> 2) * 4096, voff)
#define LOAD_A4(j) do { LOAD_A(4 * (j)); LOAD_A(4 * (j) + 1); LOAD_A(4 * (j) + 2); LOAD_A(4 * (j) + 3); } while (0)
#define LOAD_EPI() do {                                                                                       \
    dma16<0>(ws + OFF_X1 + (size_t)pb * 131072 + (size_t)(cb * 8 + w) * 2048 + eb * 512, voff + hh * 512, lbase + S3_X1 + w * 1024); } while (0)
#define LOAD_GV() do {                                                                                        \
    gld16<0>(gv, ws + OFF_GB + cb * 256, (lane & 15) * 16); } while (0)
  if (w == 7) {
    ISSUE_X(0, 0);
    BARRIER();
    {
      const u32 so = (lane >> 3) * 2048 + (lane & 7) * 64 + (((ct & 3) ^ ((lane >> 2) & 3)) * 16);
      const char* ssrc = ws + OFF_X1 + (size_t)rt * 131072 + (ct >> 2) * 512;
#pragma unroll
      for (int i = 0; i < 8; ++i) dma16<0>(ssrc + i * 16384, so, lbase + S3_SC + i * 1024);
    }
    ISSUE_X(0, 1); ISSUE_X(0, 2);
    *(u32*)(sm + S3_CT + (lane & 3) * 4) = 0u;
    const int g = lane >> 4;
    const char* sr = sm + S3_SC + (8 * g + q) * 16 + (p & 1) * 8;
    f32x4 s = {0.f, 0.f, 0.f, 0.f}, ss = s;
    const u32x4 ones = {0x3f803f80u, 0x3f803f80u, 0x3f803f80u, 0x3f803f80u};
    u32x2 pv;
    const char* fl = ws + OFF_FL + rt * 512;
#define STAT8(o) _Pragma("unroll") for (int i = 0; i < 8; ++i) {                                              \
      const u32x4 f = __builtin_shufflevector(tr_read(sr + ((o) + i) * 512), tr_read(sr + ((o) + i) * 512 + 64), 0, 1, 2, 3); \
      ss = mfma16(f, f, ss); s = mfma16(ones, f, s); }
    wait_vm<10>(); BARRIER();
    ISSUE_X(0, 3); wait_vm<2>(); BARRIER();
    ISSUE_X(0, 4); STAT8(0) SCHED(); wait_vm<2>(); BARRIER();
    ISSUE_X(0, 5); STAT8(8)
    {
      const float mean = s[0] * (1.f / D_);
      float s01 = (p & 1) ? ss[1] : ss[0], s23 = (p & 1) ? ss[3] : ss[2];
      asm("" : "+v"(s01), "+v"(s23));
      const float sq = (p & 2) ? s23 : s01;
      const float var = rbf(fmaxf(sq * (1.f / D_) - mean * mean, 0.f));
      const u32 pk = pkbf(-mean, __builtin_amdgcn_sqrtf(rbf(var + 1e-5f)));
      unsigned long long keep;
      asm volatile("s_mov_b64 %0, exec\n\ts_mov_b64 exec, 0xf0000f\n\tglobal_store_dword %1, %2, %3 sc1\n\ts_mov_b64 exec, %0"
                   : "=&s"(keep) : "v"((lane & 15) * 4), "v"(pk), "s"(fl + ct * 32) : "memory");
    }
#undef STAT8
    wait_vm<3>(); BARRIER();
    ISSUE_X(0, 6); wait_vm<3>(); BARRIER();
    ISSUE_X(0, 7); wait_vm<3>(); BARRIER();
    X2(5); wait_vm<4>(); BARRIER();
    X2(6); wait_vm<6>(); BARRIER();
    X2(7);
    asm volatile("global_load_dwordx2 %0, %1, %2 sc1\n\ts_waitcnt vmcnt(0)" : "=&v"(pv) : "v"(lane * 8), "s"(fl) : "memory");
#pragma unroll 1
    for (int spin = 0; spin < SPIN_CAP && __builtin_amdgcn_ballot_w64(pv[0] == 0u || pv[1] == 0u) != 0ull; ++spin) {
      __builtin_amdgcn_s_sleep(1);
      asm volatile("global_load_dwordx2 %0, %1, %2 sc1\n\ts_waitcnt vmcnt(0)" : "=&v"(pv) : "v"(lane * 8), "s"(fl) : "memory");
    }
    *(u32x2*)(sm + S3_ST + lane * 8) = pv;
    __syncthreads();
    LOAD_A4(0); LOAD_A4(1); LOAD_A4(2); LOAD_A4(3); LOAD_A4(4); LOAD_A4(5); LOAD_A4(6);
    LOAD_EPI(); LOAD_GV();
#pragma unroll 1
    for (int spin = 0; spin < SPIN_CAP; ++spin) {
      if (__builtin_amdgcn_readfirstlane(*(volatile LAS u32*)(size_t)(lbase + S3_CT)) >= 7u) break;
      __builtin_amdgcn_s_sleep(4);
    }
    __builtin_amdgcn_s_sleep(8);
    wait_flags(ws + OFF_CN + pb * 4096);
  } else {
    u32 xr[2][2];
#pragma unroll
    for (int i = 0; i < 2; ++i)
#pragma unroll
      for (int tt = 0; tt < 2; ++tt) {
        xr[i][tt] = lbase + i * 65536 + 1024 * hh + 64 * (4 * tt + q) + 16 * ((2 * blk + (p >> 1)) ^ (2 * hh + tt)) + 8 * (p & 1);
        asm volatile("" : "+v"(xr[i][tt]));
      }
#define XFRAG(hf, j, i) __builtin_shufflevector(tr_readl(xr[hf][0] + (2 * (j) + ((i) >> 1)) * 2048 + ((i) & 1) * 512), \
                                                tr_readl(xr[hf][1] + (2 * (j) + ((i) >> 1)) * 2048 + ((i) & 1) * 512), 0, 1, 2, 3)
    const int slab = ct * 7 + w;
    const char* wsrc = ws + OFF_WP + (size_t)slab * WKS * 1024;
    char* dst = ws + OFF_HH + ((size_t)(rt * 4) * NSLAB + slab) * 1024;
    u32x4 W[32];
    u32x2 Wc[4];
#define ISSUE_W(m) do { gld16<0>(W[4 * (m)], wsrc + (m) * 4096, voff); gld16<1024>(W[4 * (m) + 1], wsrc + (m) * 4096, voff); \
      gld16<2048>(W[4 * (m) + 2], wsrc + (m) * 4096, voff); gld16<3072>(W[4 * (m) + 3], wsrc + (m) * 4096, voff); } while (0)
#define WAIT_W(n, m) asm volatile("s_waitcnt vmcnt(%4)" : "+v"(W[4 * (m)]), "+v"(W[4 * (m) + 1]), "+v"(W[4 * (m) + 2]), \
                                  "+v"(W[4 * (m) + 3]) : "i"(n) : "memory")
    ISSUE_X(0, 0);
    gld16<0>(W[0], wsrc, voff); gld16<1024>(W[1], wsrc, voff);
    BARRIER();
    gld16<2048>(W[2], wsrc, voff); gld16<3072>(W[3], wsrc, voff);
    ISSUE_X(0, 1); ISSUE_W(1);

    f32x16 acc[4];
#pragma unroll
    for (int lt = 0; lt < 4; ++lt)
#pragma unroll
      for (int i = 0; i < 16; ++i) acc[lt][i] = 0.f;
    wait_vm2<7>(W[0], W[1]); BARRIER();
    ISSUE_X(0, 2); ISSUE_W(2);
    u32x4 f[2][4];
#define STEP(hf, j, i) do {                                                                                   \
      if ((j) > 0) acc[2 * (hf) + ((i) & 1)] = mfma32(W[(2 * (j) + 30 + ((i) >> 1)) & 31], f[((j) + 1) & 1][i], acc[2 * (hf) + ((i) & 1)]); \
      if ((j) < 16) f[(j) & 1][i] = XFRAG(hf, (j) & 15, i);                                                   \
      SCHED(); } while (0)
#define STAGE(hf, j) do { STEP(hf, j, 0); STEP(hf, j, 1); STEP(hf, j, 2); STEP(hf, j, 3); } while (0)
#define W1(s, i) do { if ((s) < 5) { gld16<(i) * 1024>(W[(4 * ((s) + 3) + (i)) & 31], wsrc + (((s) + 3) & 7) * 4096, voff); SCHED(); } } while (0)
#define X1(s, i) do { if ((s) >= 5 && 3 * ((s) - 5) + (i) < 8) { ISSUE_X(1, (3 * ((s) + 3) + (i)) & 7); SCHED(); } } while (0)
#define A2(s, i) do { if ((s) == 7) { LOAD_A(2 * (i)); LOAD_A(2 * (i) + 1); SCHED(); } } while (0)
#define SEG(s, n) do {                                                                                        \
      STEP(0, 2 * (s), 0);                                                                                    \
      if ((s) < 5) { ISSUE_X(0, ((s) + 3) & 7); SCHED(); }                                                    \
      X1(s, 0);                                                                                               \
      STEP(0, 2 * (s), 1);                                                                                    \
      STEP(0, 2 * (s), 2); W1(s, 0); X1(s, 1);                                                                \
      STEP(0, 2 * (s), 3); W1(s, 1); A2(s, 0);                                                                \
      STEP(0, 2 * (s) + 1, 0); X1(s, 2);                                                                      \
      STEP(0, 2 * (s) + 1, 1); W1(s, 2); A2(s, 1);                                                            \
      STEP(0, 2 * (s) + 1, 2); W1(s, 3); A2(s, 2);                                                            \
      if ((s) == 4) { const u32 vc = (lane & 31) * 8 + hh * 512;                                              \
        gld8<0>(Wc[0], wsrc + 32768, vc); gld8<256>(Wc[1], wsrc + 32768, vc);                                 \
        gld8<1024>(Wc[2], wsrc + 32768, vc); gld8<1280>(Wc[3], wsrc + 32768, vc); SCHED(); }                  \
      STEP(0, 2 * (s) + 1, 3);                                                                                \
      if ((s) < 7) { WAIT_W(n, ((s) + 1) & 7); if ((s) == 0) asm volatile("" : "+v"(W[2]), "+v"(W[3])); BARRIER(); } \
      SCHED(); } while (0)
    SEG(0, 10); SEG(1, 10); SEG(2, 10); SEG(3, 10); SEG(4, 14); SEG(5, 12); SEG(6, 10); SEG(7, 0);
    STAGE(0, 16);
    {
      const u32x4 mark = {~0u, ~0u, ~0u, ~0u};
      gst16s<0>(dst + (size_t)2 * NSLAB * 1024, voff, mark); gst16s<0>(dst + (size_t)3 * NSLAB * 1024, voff, mark);
    }
    LOAD_EPI();
#undef SEG
#undef W1
#undef X1
#undef A2
#undef ISSUE_W
#undef WAIT_W
    asm volatile("s_waitcnt vmcnt(%4)" : "+v"(Wc[0]), "+v"(Wc[1]), "+v"(Wc[2]), "+v"(Wc[3]) : "i"(NPRE + 3) : "memory");
    u32x4 wc;
    {
      float hi[2], mid[2], lo[2];
#pragma unroll
      for (int i = 0; i < 2; ++i) {
        const float v = add1(add1(__uint_as_float(Wc[0][i]), __uint_as_float(Wc[1][i])), add1(__uint_as_float(Wc[2][i]), __uint_as_float(Wc[3][i])));
        hi[i] = rbf(v);
        const float r1 = add1(v, -hi[i]);
        mid[i] = rbf(r1);
        lo[i] = add1(r1, -mid[i]);
      }
      wc = u32x4{pkbf(hi[0], mid[0]), pkbf(lo[0], hi[1]), pkbf(mid[1], lo[1]), 0u};
      asm volatile("" : "+v"(wc));
    }
    __syncthreads();
    u32 st[4];
#pragma unroll
    for (int lt = 0; lt < 4; ++lt) st[lt] = *(const u32*)(sm + S3_ST + (lt * 32 + (lane & 31)) * 4);
#define AUG(lt) acc[lt] = mfma32(wc, u32x4{perm_lo(st[lt], st[lt]), st[lt], perm_hi(st[lt], st[lt]), 0u}, acc[lt])
#define EPI(PK, lt) do { asm volatile("" : "+v"(acc[lt]), "+v"(st[lt]));                                       \
      gst16s<0>(dst + (size_t)(lt) * NSLAB * 1024, voff, geglu<PK>(acc[lt], st[lt])); } while (0)
#define SIGNAL(i) signal_last(lbase + S3_CT + (i) * 4, ws + OFF_CN + rt * 4096 + (i) * 2048 + ct * 128)
#define M2(j) do { STEP(1, j, 0); STEP(1, j, 1); if ((j) >= 2) LOAD_A(((j) + 4) % 28); SCHED(); STEP(1, j, 2); STEP(1, j, 3); } while (0)
    AUG(0); AUG(1); AUG(2); AUG(3);
    SCHED();
    if (w < 4) { EPI(false, 0); EPI(false, 1); }
    if (w >= 4) __builtin_amdgcn_s_setprio(1);
    M2(0); M2(1); M2(2); M2(3); M2(4);
    if (w >= 4) { EPI(false, 0); EPI(false, 1); }
    SCHED();
    M2(5);
    if (w < 4) { wait_vm<4>(); SIGNAL(0); }
    SCHED();
    M2(6); M2(7); M2(8); M2(9); M2(10);
    if (w >= 4) { wait_vm<6>(); SIGNAL(0); }
    SCHED();
    M2(11); M2(12); M2(13); M2(14); M2(15); M2(16);
    LOAD_A(21); LOAD_A(22); LOAD_A(23); LOAD_A4(6);
    LOAD_GV();
    if (w >= 4) {
      EPI(false, 2); EPI(false, 3);
      __builtin_amdgcn_s_setprio(0);
    }
    if (w < 4) { EPI(true, 2); EPI(true, 3); }
#undef M2
#undef EPI
#undef AUG
#undef STAGE
#undef STEP
#undef XFRAG
  }
#undef ISSUE_X
#undef X2
#undef LOAD_A
#undef LOAD_A4
#undef LOAD_EPI
#undef LOAD_GV
  BARRIER();
  const char* hsrc = ws + OFF_HH + ((size_t)(4 * pb + eb) * NSLAB + w * 14) * 1024;
  u32x4 H[NW];
#define HBASE(n) (hsrc + ((n) >= 14 ? 2 * NSLAB * 1024 : 0) + (((n) % 14) >> 2) * 4096)
#define LOAD_H(n) HReg<(n) % NW>::load<(((n) % 14) & 3) * 1024>(H[(n) % NW], HBASE(n), voff)
  LOAD_H(0); LOAD_H(1); LOAD_H(2); LOAD_H(3); LOAD_H(4); LOAD_H(5);
  u32 ra, ov;
  f32x4 xres[2];
  {
    const int l = 4 * w + (lane >> 4), ch = (lane >> 1) & 7, h = lane & 1;
    ra = lbase + ch * EXC + h * 528 + l * 16;
    ov = ((lane >> 4) + h * 64) * (D_ * 2) + ch * 16;
    const char* xb = sm + S3_X1 + ch * 1024 + h * 256 + (((w >> 1) ^ (lane & 3)) * 16) + (l & 7) * 2;
#pragma unroll
    for (int i = 0; i < 8; ++i) xres[i >> 2][i & 3] = bf(*(const u16*)(xb + (i >> 2) * 512 + (i & 3) * 64));
  }
  char* op = (char*)out + ((size_t)(pb * 128 + 32 * eb + 4 * w) * D_ + cb * 64) * 2;
  u32 rb = ra + EXT;
  asm volatile("" : "+v"(ra), "+v"(rb), "+v"(ov), "+v"(xres[0]), "+v"(xres[1]), "+s"(op));
  f32x16 acc[2][2];
#pragma unroll
  for (int c = 0; c < 4; ++c)
#pragma unroll
    for (int i = 0; i < 16; ++i) acc[c >> 1][c & 1][i] = 0.f;
  asm volatile("s_waitcnt vmcnt(%15)" : "+v"(gv), "+v"(A[0]), "+v"(A[1]), "+v"(A[2]), "+v"(A[3]), "+v"(A[4]), "+v"(A[5]), "+v"(A[6]),
               "+v"(A[7]), "+v"(A[8]), "+v"(A[9]), "+v"(A[10]), "+v"(A[11]), "+v"(A[12]), "+v"(A[13]) : "i"(NW) : "memory");
  asm volatile("" : "+v"(A[14]), "+v"(A[15]), "+v"(A[16]), "+v"(A[17]), "+v"(A[18]), "+v"(A[19]), "+v"(A[20]), "+v"(A[21]),
               "+v"(A[22]), "+v"(A[23]), "+v"(A[24]), "+v"(A[25]), "+v"(A[26]), "+v"(A[27]) :: "memory");
#define BSTEP(n) do {                                                                                         \
    if ((n) < 14) HReg<(n) % NW>::wait<NW - 1>(H[(n) % NW]);                                                  \
    else HReg<(n) % NW>::wait_checked<(27 - (n) < NW - 1 ? 27 - (n) : NW - 1), (((n) % 14) & 3) * 1024>(H[(n) % NW], HBASE(n), voff); \
    acc[(n) / 14][0] = mfma32(A[2 * ((n) % 14)], H[(n) % NW], acc[(n) / 14][0]);                              \
    acc[(n) / 14][1] = mfma32(A[2 * ((n) % 14) + 1], H[(n) % NW], acc[(n) / 14][1]);                          \
    SCHED();                                                                                                  \
    if ((n) + NW < 28) LOAD_H(((n) + NW) % 28);                                                               \
    SCHED(); } while (0)
#define PUT(c) do { char* rp = sm + (c) * EXT + w * EXS + (lane + hh) * 16;                                   \
    _Pragma("unroll") for (int i = 0; i < 8; ++i)                                                             \
      *(f32x4*)(rp + i * EXC) = f32x4{acc[c][i >> 2][4 * (i & 3)], acc[c][i >> 2][4 * (i & 3) + 1],           \
                                      acc[c][i >> 2][4 * (i & 3) + 2], acc[c][i >> 2][4 * (i & 3) + 3]};      \
    asm volatile("s_waitcnt lgkmcnt(0)" ::: "memory"); } while (0)
  BSTEP(0); BSTEP(1); BSTEP(2); BSTEP(3); BSTEP(4); BSTEP(5); BSTEP(6);
  BSTEP(7); BSTEP(8); BSTEP(9); BSTEP(10); BSTEP(11); BSTEP(12); BSTEP(13);
  BSTEP(14);
  PUT(0);
  BSTEP(15); BSTEP(16);
  BSTEP(17); BSTEP(18); BSTEP(19); BSTEP(20); BSTEP(21);
  BSTEP(22); BSTEP(23); BSTEP(24); BSTEP(25); BSTEP(26); BSTEP(27);
  PUT(1);
  BARRIER();
  {
    f32x4 v0[8], v1[8];
#pragma unroll
    for (int i = 0; i < 8; ++i) { v0[i] = *(const LAS f32x4*)(size_t)(ra + i * EXS); v1[i] = *(const LAS f32x4*)(size_t)(rb + i * EXS); }
    SCHED();
    const u32x2 r0 = finish_tile(v0, xres[0], gv), r1 = finish_tile(v1, xres[1], gv);
    const bool odd = lane & 1;
    const u32 e0 = __builtin_amdgcn_mov_dpp(odd ? r0[0] : r1[0], 0xB1, 0xf, 0xf, true);
    const u32 e1 = __builtin_amdgcn_mov_dpp(odd ? r0[1] : r1[1], 0xB1, 0xf, 0xf, true);
    gst16s<0>(op, ov,
              odd ? u32x4{e0, e1, r1[0], r1[1]} : u32x4{r0[0], r0[1], e0, e1});
  }
#undef PUT
#undef BSTEP
#undef LOAD_H
#undef HBASE
#undef SIGNAL
}

extern "C" __global__ void adaln_end() {}

extern "C" void kernel_launch(void* const* d_in, const int* in_sizes, int n_in, void* d_out, int out_size,
                              void* d_ws, size_t ws_size, hipStream_t stream) {
  char* ws = (char*)d_ws;
  (void)hipFuncSetAttribute((const void*)adaln_k12, hipFuncAttributeMaxDynamicSharedMemorySize, S2_END);
  (void)hipFuncSetAttribute((const void*)adaln_k34, hipFuncAttributeMaxDynamicSharedMemorySize, S3_END);
  adaln_k12<<<256, 512, S2_END, stream>>>((const u16*)d_in[0], (const u16*)d_in[1], (const u16*)d_in[2], (const u16*)d_in[3],
                                          (const u16*)d_in[4], (const u16*)d_in[5], (const u16*)d_in[6], (const u16*)d_in[8],
                                          (const u16*)d_in[9], (const u16*)d_in[10], (const u16*)d_in[11], (const u16*)d_in[12], ws);
  adaln_k34<<<256, 512, S3_END, stream>>>(ws, (u16*)d_out);
}
```
